```python
import torch

def _softmax(x, axis=-1):
    return torch.softmax(x, dim=axis)
def _gelu(x):
    return torch.nn.functional.gelu(x, approximate="tanh")
def _silu(x):
    return torch.nn.functional.silu(x)
def _sigmoid(x):
    return torch.sigmoid(x)
def _layernorm(x, axis=-1, eps=1e-5):
    m = x.mean(dim=axis, keepdim=True)
    v = x.var(dim=axis, keepdim=True, unbiased=False)
    return (x - m) / torch.sqrt(v + eps)
def _scatter_add(tgt, idx, src, axis=0):
    return tgt.clone().index_add_(axis, idx, src)
def _segment_sum(src, seg, n):
    out = torch.zeros((n,) + src.shape[1:], dtype=src.dtype)
    return out.index_add_(0, seg, src)
def _pad(x, pw):
    flat = tuple(v for pair in reversed(pw) for v in pair)
    return torch.nn.functional.pad(x, flat)

kernel_name = 'parallel_sum__ssm_conv_s4d__glu_gated_swiglu__prompt'

def setup_inputs():

    _w = (lambda a: (lambda t: t.to(torch.bfloat16) if t.is_floating_point() else t)(torch.as_tensor(a)))
    def _A_setup():
        Bsz, L, D, N = 1, 2048, 640, 128
        return {
            "u": _w((lambda *s: torch.randn(*s, dtype=torch.bfloat16) * 0.05)(Bsz, L, D)),
            "log_A_real": _w((lambda *s: torch.randn(*s, dtype=torch.bfloat16) * 3.2)(D, N) * 0.5),
            "A_imag": _w((lambda *s: torch.randn(*s, dtype=torch.bfloat16) * 0.05)(D, N) * 3.14),
            "C_real": _w((lambda *s: torch.randn(*s, dtype=torch.bfloat16) * 0.2)(D, N) / N ** 0.5),
            "C_imag": _w((lambda *s: torch.randn(*s, dtype=torch.bfloat16) * 0.2)(D, N) / N ** 0.5),
            "log_dt": _w((lambda *s: torch.randn(*s, dtype=torch.bfloat16) * 0.05)(D) * 0.5 - 4.0),
            "D_skip": _w((lambda *s: torch.randn(*s, dtype=torch.bfloat16) * 0.05)(D)),
        }
    _A_in = _A_setup()

    def _B_setup():
        B, S, D, F = 1, 2048, 640, 1728
        return {
            "x": _w((lambda *s: torch.randn(*s, dtype=torch.bfloat16) * 0.05)(B, S, D)),
            "w1": _w((lambda *s: torch.randn(*s, dtype=torch.bfloat16) * 0.4)(D, F) * (2.0 / D) ** 0.5),
            "w3": _w((lambda *s: torch.randn(*s, dtype=torch.bfloat16) * 0.4)(D, F) * (2.0 / D) ** 0.5),
            "w2": _w((lambda *s: torch.randn(*s, dtype=torch.bfloat16) * 0.4)(F, D) * (2.0 / F) ** 0.5),
        }
    _B_in = _B_setup()

    return {
        "A_u": _A_in["u"],
        "A_log_A_real": _A_in["log_A_real"],
        "A_A_imag": _A_in["A_imag"],
        "A_C_real": _A_in["C_real"],
        "A_C_imag": _A_in["C_imag"],
        "A_log_dt": _A_in["log_dt"],
        "A_D_skip": _A_in["D_skip"],
        "B_x": _B_in["x"],
        "B_w1": _B_in["w1"],
        "B_w3": _B_in["w3"],
        "B_w2": _B_in["w2"],
    }

def reference(A_u, A_log_A_real, A_A_imag, A_C_real, A_C_imag, A_log_dt, A_D_skip, B_x, B_w1, B_w3, B_w2):
    def _A_ref(u, log_A_real, A_imag, C_real, C_imag, log_dt, D_skip):
        L = u.shape[1]
        dt = torch.exp(log_dt)
        A = -torch.exp(log_A_real) + 1j * A_imag
        C = (C_real + 1j * C_imag) * dt[:, None]
        pos = (lambda x, dt: x.to(dt))(torch.arange(L), log_dt.dtype)
        P = torch.exp((A * dt[:, None])[:, :, None] * pos[None, None, :])
        K = 2.0 * torch.einsum("dn,dnl->dl", C, P).real
        L2 = 2 * L
        u_t = (lambda x, *d: x.permute(*d))(u, 0, 2, 1)
        Kf = (lambda x, *a, **k: torch.fft.rfft(x.float(), *a, **k))(K, L2)
        Uf = (lambda x, *a, **k: torch.fft.rfft(x.float(), *a, **k))(u_t, L2)
        y = (lambda x, *a, **k: torch.fft.irfft(x, *a, **k).to(torch.bfloat16))(Uf * Kf[None, :, :], L2)[:, :, :L]
        y = (lambda x, *d: x.permute(*d))(y, 0, 2, 1)
        return y + u * D_skip[None, None, :]

    def _B_ref(x, w1, w3, w2):
        gate = torch.einsum("bsd,df->bsf", x, w1)
        up = torch.einsum("bsd,df->bsf", x, w3)
        h = _silu(gate) * up
        out = torch.einsum("bsf,fd->bsd", h, w2)
        return out

    x = A_u
    xn = _layernorm(x, axis=-1)
    return x + _A_ref(xn, A_log_A_real, A_A_imag, A_C_real, A_C_imag, A_log_dt, A_D_skip) + _B_ref(xn, B_w1, B_w3, B_w2)
```

```cpp
#include <hip/hip_runtime.h>
#include <hip/hip_bf16.h>
#include <stdint.h>

typedef unsigned short u16;
typedef unsigned u32;
typedef u32 u32x2 __attribute__((ext_vector_type(2)));
typedef u32 u32x4 __attribute__((ext_vector_type(4)));
typedef float f32x2 __attribute__((ext_vector_type(2)));
typedef float f32x4 __attribute__((ext_vector_type(4)));
typedef float f32x16 __attribute__((ext_vector_type(16)));
typedef short s16x4 __attribute__((ext_vector_type(4)));
typedef short s16x8 __attribute__((ext_vector_type(8)));
typedef __bf16 bf16x8 __attribute__((ext_vector_type(8)));
typedef _Float16 f16x8 __attribute__((ext_vector_type(8)));
typedef _Float16 f16x2 __attribute__((ext_vector_type(2)));
#define LAS __attribute__((address_space(3)))
#define DEV __device__ __forceinline__

constexpr int L_ = 2048, D_ = 640, F_ = 1728, N_ = 128;

constexpr size_t WS_XNP = 0;
constexpr size_t WS_KR  = WS_XNP + (size_t)L_ * D_ * 2;
constexpr size_t WS_KSC = WS_KR + (size_t)D_ * 2080 * 2;
constexpr size_t WS_W2P = WS_KSC + 4096;
constexpr size_t WS_H   = WS_W2P + (size_t)F_ * D_ * 2;
constexpr size_t WS_YT  = WS_H + (size_t)L_ * F_ * 2;
constexpr size_t WS_END = WS_YT + (size_t)L_ * D_ * 2;

DEV float bf2f(u32 b) { return __uint_as_float(b << 16); }
DEV float bflo(u32 w) { return __uint_as_float(w << 16); }
DEV float bfhi(u32 w) { return __uint_as_float(w & 0xffff0000u); }
DEV u32 cvt_pk_bf16(float lo, float hi) { u32 r; asm("v_cvt_pk_bf16_f32 %0, %1, %2" : "=v"(r) : "v"(lo), "v"(hi)); return r; }
DEV float rbf(float x) { return bflo(cvt_pk_bf16(x, x)); }
DEV u32 f16_pk(float lo, float hi) { f16x2 v = {(_Float16)lo, (_Float16)hi}; return __builtin_bit_cast(u32, v); }

constexpr int PREP_LN_BLOCKS = 128, PREP_KG_BLOCKS = 320, PREP_W2_BLOCKS = 54;
constexpr int KG_ROW = 528;
constexpr int KG_CH_LDS = (21 + 32) * KG_ROW + 1344;
constexpr int PREP_LDS = 2 * KG_CH_LDS;

DEV float row16_sum(float v) {
    v += __shfl_xor(v, 1); v += __shfl_xor(v, 2); v += __shfl_xor(v, 4); v += __shfl_xor(v, 8); return v;
}

DEV void prep_ln(int b, const u16* __restrict__ x, u16* __restrict__ xnp) {
    const int tid = threadIdx.x, lane = tid & 63, wave = tid >> 6;
    const int r = b * 16 + wave * 4 + (lane >> 4), l16 = lane & 15;
    u32x4 v[5];
#pragma unroll
    for (int i = 0; i < 5; ++i) v[i] = *(const u32x4*)(x + (size_t)r * D_ + (l16 + 16 * i) * 8);
    float s = 0.f;
#pragma unroll
    for (int i = 0; i < 5; ++i)
#pragma unroll
        for (int j = 0; j < 4; ++j) s += bflo(v[i][j]) + bfhi(v[i][j]);
    s = row16_sum(s);
    const float mean = s * (1.0f / D_);
    float q = 0.f;
#pragma unroll
    for (int i = 0; i < 5; ++i)
#pragma unroll
        for (int j = 0; j < 4; ++j) { float a = bflo(v[i][j]) - mean, c = bfhi(v[i][j]) - mean; q += a * a + c * c; }
    q = row16_sum(q);
    const float m = rbf(mean);
    const float var = rbf(q * (1.0f / D_));
    const float sd = rbf(sqrtf(rbf(var + 1e-5f)));
#pragma unroll
    for (int i = 0; i < 5; ++i) {
        u32x4 o;
#pragma unroll
        for (int j = 0; j < 4; ++j) {
            u32 d = cvt_pk_bf16(bflo(v[i][j]) - m, bfhi(v[i][j]) - m);
            o[j] = cvt_pk_bf16(bflo(d) / sd, bfhi(d) / sd);
        }
        const int chi = l16 + 16 * i;
        *(u32x4*)(xnp + ((size_t)((r >> 5) * 40 + (chi >> 1)) * 64 + (r & 31) + 32 * (chi & 1)) * 8) = o;
    }
}

struct cf { float re, im; };
DEV cf cmul(cf a, cf b) { return cf{a.re * b.re - a.im * b.im, a.re * b.im + a.im * b.re}; }
DEV cf cexp_p(float e2, float tr, float p) {
    float mag = __builtin_amdgcn_exp2f(e2 * p);
    float th = __builtin_amdgcn_fractf(tr * p);
    return cf{mag * __builtin_amdgcn_cosf(th), mag * __builtin_amdgcn_sinf(th)};
}

DEV void prep_kgen(int b, char* smem, const u16* __restrict__ logA, const u16* __restrict__ Aim, const u16* __restrict__ Cre,
                   const u16* __restrict__ Cim, const u16* __restrict__ logdt, u16* __restrict__ kr, float* __restrict__ ksc) {
    const int tid = threadIdx.x, lane = tid & 63, wave = __builtin_amdgcn_readfirstlane(tid >> 6);
    const int ch = b * 2 + (wave >> 1), half = wave & 1, n = half * 64 + lane;
    char* base = smem + (wave >> 1) * KG_CH_LDS;
    char* Lm = base; char* Rm = base + 21 * KG_ROW; u16* T = (u16*)(base + 53 * KG_ROW);
    const float dt = rbf(expf(bf2f(logdt[ch])));
    const float are = -rbf(expf(bf2f(logA[ch * N_ + n])));
    const float zr = are * dt, zi = bf2f(Aim[ch * N_ + n]) * dt;
    const float cr = bf2f(Cre[ch * N_ + n]) * dt, ci = bf2f(Cim[ch * N_ + n]) * dt;
    float mx = fmaxf(fabsf(cr), fabsf(ci));
    { const int n2 = n ^ 64; mx = fmaxf(mx, fmaxf(fabsf(bf2f(Cre[ch * N_ + n2]) * dt), fabsf(bf2f(Cim[ch * N_ + n2]) * dt))); }
#pragma unroll
    for (int o = 1; o < 64; o <<= 1) mx = fmaxf(mx, __shfl_xor(mx, o));
    int ex = (int)((__float_as_uint(mx) >> 23) & 0xff);
    ex = ex < 1 ? 127 : (ex > 253 ? 253 : ex);
    const float sig = __uint_as_float((u32)(254 - ex) << 23);
    const float isig = __uint_as_float((u32)ex << 23);
    const float e2 = zr * 1.4426950408889634f, tr = zi * 0.15915494309189535f;
    const cf w1 = cexp_p(e2, tr, 1.f), w8 = cexp_p(e2, tr, 8.f), w128 = cexp_p(e2, tr, 128.f), q = cexp_p(e2, tr, 256.f);
    auto putR = [&](int j, cf v) { *(u32*)(Rm + j * KG_ROW + n * 4) = f16_pk(v.re, v.im); };
    auto putL = [&](int i, cf v) { *(u32*)(Lm + i * KG_ROW + n * 4) = f16_pk(v.re, -v.im); };
    {
        cf r{1.f, 0.f};
#pragma unroll
        for (int j = 0; j < 16; ++j) { putR(j, r); putR(j + 16, cmul(r, w128)); if (j < 15) r = cmul(r, w8); }
    }
    {
        cf l[8]; l[0] = cf{cr * sig, ci * sig};
#pragma unroll
        for (int i = 1; i < 8; ++i) l[i] = cmul(l[i - 1], w1);
#pragma unroll
        for (int i = 0; i < 8; ++i) putL(i, l[i]);
#pragma unroll
        for (int i = 0; i < 4; ++i) putL(8 + i, cmul(q, l[2 * i]));
        const cf q2 = cmul(q, q), q3 = cmul(q2, q);
        const cf a = cmul(q2, l[0]);
        putL(12, a); putL(13, cmul(q2, l[4])); putL(14, cmul(q3, l[0])); putL(15, cmul(q3, l[4]));
        cf c = cmul(q2, a);
#pragma unroll
        for (int i = 0; i < 5; ++i) { putL(16 + i, c); if (i < 4) c = cmul(c, q); }
    }
    __syncthreads();
    if (half == 0) {
        f32x16 acc = {};
        const int i = lane & 31, h = lane >> 5;
#pragma unroll
        for (int ks = 0; ks < 16; ++ks) {
            f16x8 a = *(const f16x8*)(Lm + i * KG_ROW + 32 * ks + 16 * h);
            f16x8 bb = *(const f16x8*)(Rm + i * KG_ROW + 32 * ks + 16 * h);
            acc = __builtin_amdgcn_mfma_f32_32x32x16_f16(a, bb, acc, 0, 0, 0);
        }
        const int j = i;
#pragma unroll
        for (int reg = 0; reg < 12; ++reg) {
            const int r3 = reg & 3, g = reg >> 2;
            int idx;
            if (g == 0) idx = r3 + 4 * h + 8 * j;
            else if (g == 1) idx = h == 0 ? 256 + r3 + 4 * j : (384 + 64 * (r3 >> 1) + (r3 & 1) + 2 * j);
            else idx = h == 0 ? 512 + 32 * r3 + j : 640;
            const bool ok = g < 2 || h == 0 || (r3 == 0 && j == 0);
            const _Float16 v = (_Float16)acc[reg];
            if (ok) T[idx] = __builtin_bit_cast(u16, v);
        }
        if (lane == 0) ksc[ch] = 2.0f * isig;
    }
    __syncthreads();
    for (int c = half * 64 + lane; c < 260; c += 128) {
        u32x4 o;
#pragma unroll
        for (int jj = 0; jj < 4; ++jj) {
            u32 pr[2];
#pragma unroll
            for (int e = 0; e < 2; ++e) {
                const int t = 8 * c + 2 * jj + e, l = 2048 - t;
                const u32 bits = cvt_pk_bf16((float)l, (float)l) & 0xffffu;
                int idx = l < 128 ? l : (int)bits - 17024;
                const bool ok = t >= 1 && t <= 2048;
                idx = ok ? idx : 0;
                const u32 v = T[idx];
                pr[e] = ok ? v : 0u;
            }
            o[jj] = pr[0] | (pr[1] << 16);
        }
        *(u32x4*)(kr + (size_t)ch * 2080 + 8 * c) = o;
    }
}

DEV void prep_w2(int ks, char* smem, const u16* __restrict__ w2, u16* __restrict__ w2p) {
    const int tid = threadIdx.x;
    u16* S = (u16*)smem;
    constexpr int RS = 648;
    for (int c = tid; c < 32 * 80; c += 256) {
        const int row = c / 80, col = c % 80;
        *(u32x4*)(S + row * RS + col * 8) = *(const u32x4*)(w2 + (size_t)(ks * 32 + row) * D_ + col * 8);
    }
    __syncthreads();
    for (int p = tid; p < 40 * 64; p += 256) {
        const int nt = p >> 6, lane = p & 63, n = nt * 16 + (lane & 15), kq = lane >> 4;
        u32x4 o;
#pragma unroll
        for (int jj = 0; jj < 4; ++jj) {
            const int j0 = 2 * jj;
            const int f0 = 16 * (kq >> 1) + 4 * (kq & 1) + (j0 & 3) + 8 * (j0 >> 2);
            o[jj] = (u32)S[f0 * RS + n] | ((u32)S[(f0 + 1) * RS + n] << 16);
        }
        *(u32x4*)(w2p + ((size_t)(nt * 54 + ks) * 64 + lane) * 8) = o;
    }
}

__global__ __launch_bounds__(256) void k_prep(const u16* x, const u16* logA, const u16* Aim, const u16* Cre, const u16* Cim,
                                              const u16* logdt, const u16* w2, char* ws) {
    extern __shared__ __attribute__((aligned(16))) char smem[];
    const int b = blockIdx.x;
    if (b < PREP_KG_BLOCKS) prep_kgen(b, smem, logA, Aim, Cre, Cim, logdt, (u16*)(ws + WS_KR), (float*)(ws + WS_KSC));
    else if (b < PREP_KG_BLOCKS + PREP_LN_BLOCKS) prep_ln(b - PREP_KG_BLOCKS, x, (u16*)(ws + WS_XNP));
    else prep_w2(b - PREP_KG_BLOCKS - PREP_LN_BLOCKS, smem, w2, (u16*)(ws + WS_W2P));
}

constexpr int G1_UOFF = 14336 + 128, G1_STAGE = G1_UOFF + 14336, G1_NST = 5, G1_LDS = G1_STAGE * G1_NST;
constexpr int G1_NT = 10, G1_XD = 8;

constexpr int g1_cnt_before_iter(int t) {
    int n = 7 * 4 + G1_XD;
    for (int i = 0; i < t; ++i) { if (i + 4 < G1_NT) n += 7; for (int s = 4 * i; s < 4 * i + 4; ++s) if (s + G1_XD < 40) n += 1; }
    return n;
}
constexpr int g1_idx_w(int t) {
    if (t == 0) return 7; if (t == 1) return 7 + 4 + 7; if (t == 2) return 7 + 4 + 7 + 4 + 7; if (t == 3) return 36;
    return g1_cnt_before_iter(t - 4) + 7;
}
constexpr int g1_idx_x(int s) {
    if (s < 4) return 7 + s + 1; if (s < 8) return 7 + 4 + 7 + (s - 4) + 1;
    const int s0 = s - G1_XD, t = s0 / 4;
    int n = g1_cnt_before_iter(t) + (t + 4 < G1_NT ? 7 : 0);
    for (int i = 4 * t; i <= s0; ++i) if (i + G1_XD < 40) n += 1;
    return n;
}
constexpr int g1_cnt_at_step(int s) {
    const int t = s / 4;
    int n = g1_cnt_before_iter(t) + (t + 4 < G1_NT ? 7 : 0);
    for (int i = 4 * t; i <= s; ++i) if (i + G1_XD < 40) n += 1;
    return n;
}

DEV void glds16(u32 voff, const void* sbase, u32 lds_dst) {
    u32 keep;
    asm volatile("s_mov_b32 %0, m0\n\ts_mov_b32 m0, %3\n\ts_nop 0\n\tglobal_load_lds_dwordx4 %1, %2\n\ts_mov_b32 m0, %0" : "=&s"(keep) : "v"(voff), "s"(sbase), "s"(lds_dst) : "memory");
}
template <int OFF> DEV void gload16(u32x4& dst, u32 voff, const void* sbase) {
    asm volatile("global_load_dwordx4 %0, %1, %2 offset:%3" : "=v"(dst) : "v"(voff), "s"(sbase), "i"(OFF));
}
template <int N> DEV void wait_vm_reg(u32x4& r) { asm volatile("s_waitcnt vmcnt(%1)" : "+v"(r) : "i"(N)); }
template <int N> DEV void wait_vm() { asm volatile("s_waitcnt vmcnt(%0)" :: "i"(N) : "memory"); }
DEV s16x4 tr_read(u32 addr) { return __builtin_bit_cast(s16x4, __builtin_amdgcn_ds_read_tr16_b64_v4i16((LAS s16x4*)(size_t)addr)); }

template <int I, int N, class Fn> DEV void static_for(Fn&& f) { if constexpr (I < N) { f(std::integral_constant<int, I>{}); static_for<I + 1, N>(f); } }

__global__ __launch_bounds__(256) void k_gemm1(const u16* __restrict__ w1, const u16* __restrict__ w3, char* ws) {
    extern __shared__ __attribute__((aligned(16))) char smem[];
    const int tid = threadIdx.x, lane = tid & 63, wave = __builtin_amdgcn_readfirstlane(tid >> 6);
    const int b = blockIdx.x, xcd = b & 7, idx = b >> 3;
    const int mt = (xcd & 1) * 8 + (idx & 7), ft = (xcd >> 1) * 4 + (idx >> 3);
    const int f0 = ft * 112;
    const int nvc = (F_ - f0) >= 112 ? 14 : (F_ - f0) / 8;
    const u32 lds0 = (u32)(size_t)(LAS char*)smem;
    u32 voff[7]; const char* sb[7]; u32 ldst[7];
#pragma unroll
    for (int i = 0; i < 7; ++i) {
        const int g = wave + 4 * i, up = g >= 14, p = up ? g - 14 : g;
        const int c = 64 * p + lane, row = c / 14; int col = c % 14; col = col < nvc ? col : nvc - 1;
        voff[i] = row * (F_ * 2) + col * 16;
        sb[i] = (const char*)(up ? w3 : w1) + f0 * 2;
        ldst[i] = lds0 + (up ? G1_UOFF : 0) + p * 1024;
    }
    auto issue_w = [&](auto tc) {
        constexpr int t = decltype(tc)::value;
#pragma unroll
        for (int i = 0; i < 7; ++i) glds16(voff[i], sb[i] + (size_t)t * 64 * F_ * 2, ldst[i] + (t % G1_NST) * G1_STAGE);
    };
    const char* xb = ws + WS_XNP + (size_t)(mt * 4 + wave) * 40 * 1024;
    const u32 xoff = lane * 16;
    u32x4 xf[40];
    auto issue_x = [&](auto sc) { constexpr int s = decltype(sc)::value; gload16<(s & 3) * 1024>(xf[s], xoff, xb + (s >> 2) * 4096); };
    using std::integral_constant;
#define IC(n) std::integral_constant<int, n>{}
    issue_w(IC(0)); static_for<0, 4>(issue_x); issue_w(IC(1)); static_for<4, 8>(issue_x); issue_w(IC(2)); issue_w(IC(3));

    const u32 tb = lds0 + ((lane >> 4) & 1) * G1_UOFF + (8 * (lane >> 5) + ((lane & 15) >> 2)) * 224 + 8 * (lane & 3);
    f32x16 acc[7];
#pragma unroll
    for (int T = 0; T < 7; ++T) acc[T] = f32x16{};
    s16x4 wl[2][7], wh[2][7];
    auto read_w = [&](auto sc) {
        constexpr int s = decltype(sc)::value, t = s / 4, ks = s % 4, cur = s & 1;
        const u32 a = tb + (t % G1_NST) * G1_STAGE + 16 * ks * 224;
#pragma unroll
        for (int T = 0; T < 7; ++T) { wl[cur][T] = tr_read(a + 32 * T); wh[cur][T] = tr_read(a + 4 * 224 + 32 * T); }
    };
    static_for<0, G1_NT>([&](auto tc) {
        constexpr int t = decltype(tc)::value;
        constexpr int need = t + 1 < G1_NT ? t + 1 : G1_NT - 1;
        constexpr int nw = g1_cnt_before_iter(t) - g1_idx_w(need);
        asm volatile("s_waitcnt lgkmcnt(14)" ::: "memory");
        wait_vm<(nw < 0 ? 0 : nw)>();
        __builtin_amdgcn_s_barrier();
        asm volatile("" ::: "memory");
        if constexpr (t + 4 < G1_NT) issue_w(IC(t + 4));
        if constexpr (t == 0) read_w(IC(0));
        static_for<4 * t, 4 * t + 4>([&](auto sc) {
            constexpr int s = decltype(sc)::value, cur = s & 1;
            if constexpr (s + G1_XD < 40) issue_x(IC(s + G1_XD));
            wait_vm_reg<g1_cnt_at_step(s) - g1_idx_x(s)>(xf[s]);
            if constexpr (s + 1 < 40) read_w(IC(s + 1));
            const bf16x8 xb8 = __builtin_bit_cast(bf16x8, xf[s]);
#pragma unroll
            for (int T = 0; T < 7; ++T) {
                const s16x8 w = __builtin_shufflevector(wl[cur][T], wh[cur][T], 0, 1, 2, 3, 4, 5, 6, 7);
                acc[T] = __builtin_amdgcn_mfma_f32_32x32x16_bf16(__builtin_bit_cast(bf16x8, w), xb8, acc[T], 0, 0, 0);
            }
#pragma unroll
            for (int T = 0; T < 7; ++T) {
                __builtin_amdgcn_sched_group_barrier(0x8, 1, 0);
                __builtin_amdgcn_sched_group_barrier(0x100, 2, 0);
            }
            __builtin_amdgcn_sched_barrier(0);
        });
    });
    const int r32 = lane & 31, hh = lane >> 5;
    const int rt16 = (mt * 128 + wave * 32 + r32) >> 4;
    u16* hp = (u16*)(ws + WS_H);
#pragma unroll
    for (int T = 0; T < 7; ++T) {
        const int Tg = ft * 7 + T;
        u32x4 o;
#pragma unroll
        for (int j = 0; j < 4; ++j) {
            const u32 gp = cvt_pk_bf16(acc[T][2 * j], acc[T][2 * j + 1]);
            const u32 upk = cvt_pk_bf16(acc[T][8 + 2 * j], acc[T][8 + 2 * j + 1]);
            const float g0 = bflo(gp), g1 = bfhi(gp);
            const float s0 = g0 * __builtin_amdgcn_rcpf(1.f + __builtin_amdgcn_exp2f(g0 * -1.4426950408889634f));
            const float s1 = g1 * __builtin_amdgcn_rcpf(1.f + __builtin_amdgcn_exp2f(g1 * -1.4426950408889634f));
            const u32 sp = cvt_pk_bf16(s0, s1);
            o[j] = cvt_pk_bf16(bflo(sp) * bflo(upk), bfhi(sp) * bfhi(upk));
        }
        if (Tg < 108) *(u32x4*)(hp + ((size_t)(rt16 * 54 + (Tg >> 1)) * 64 + (r32 & 15) + 16 * (2 * (Tg & 1) + hh)) * 8) = o;
    }
}

constexpr int CV_COPY = 4160, CV_U = 4608, CV_WAVE = 4 * CV_COPY + CV_U, CV_LDS = 4 * CV_WAVE;

__global__ __launch_bounds__(256) void k_conv(char* ws) {
    extern __shared__ __attribute__((aligned(16))) char smem[];
    const int tid = threadIdx.x, lane = tid & 63, wave = __builtin_amdgcn_readfirstlane(tid >> 6);
    const int c0 = blockIdx.x * 4, ch = c0 + wave;
    const u16* xnp = (const u16*)(ws + WS_XNP);
    const u16* kr = (const u16*)(ws + WS_KR) + (size_t)ch * 2080;
    char* my = smem + wave * CV_WAVE;
    {
        u32x4 v[5]; u32x2 p[5];
#pragma unroll
        for (int i = 0; i < 5; ++i) {
            int c = lane + 64 * i; c = c < 260 ? c : 259;
            v[i] = *(const u32x4*)(kr + 8 * c);
            p[i] = *(const u32x2*)(kr + (c > 0 ? 8 * c - 4 : 0));
        }
#pragma unroll
        for (int i = 0; i < 5; ++i) {
            const int c = lane + 64 * i;
            if (c < 260) {
                *(u32x4*)(my + 16 * c) = v[i];
                *(u32x4*)(my + CV_COPY + 16 * c) = u32x4{__builtin_amdgcn_alignbit(v[i].x, p[i].y, 16), __builtin_amdgcn_alignbit(v[i].y, v[i].x, 16),
                                                      __builtin_amdgcn_alignbit(v[i].z, v[i].y, 16), __builtin_amdgcn_alignbit(v[i].w, v[i].z, 16)};
                *(u32x4*)(my + 2 * CV_COPY + 16 * c) = u32x4{p[i].y, v[i].x, v[i].y, v[i].z};
                *(u32x4*)(my + 3 * CV_COPY + 16 * c) = u32x4{__builtin_amdgcn_alignbit(p[i].y, p[i].x, 16), __builtin_amdgcn_alignbit(v[i].x, p[i].y, 16),
                                                          __builtin_amdgcn_alignbit(v[i].y, v[i].x, 16), __builtin_amdgcn_alignbit(v[i].z, v[i].y, 16)};
            }
        }
    }
    {
        const int ks = c0 >> 4, hf = (c0 >> 3) & 1, j0 = c0 & 7;
        u32x2 a[4], bq[4];
#pragma unroll
        for (int it = 0; it < 4; ++it) {
            const int r = it * 512 + 2 * tid;
            const u16* pa = xnp + ((size_t)((r >> 5) * 40 + ks) * 64 + (r & 31) + 32 * hf) * 8 + j0;
            a[it] = *(const u32x2*)pa; bq[it] = *(const u32x2*)(pa + 8);
        }
        if (tid < 128) *(u32x4*)(smem + (tid >> 5) * CV_WAVE + 4 * CV_COPY + (tid & 31) * 16) = u32x4{0, 0, 0, 0};
#pragma unroll
        for (int it = 0; it < 4; ++it) {
            const int r = it * 512 + 2 * tid;
            char* up = smem + 4 * CV_COPY + (256 + r) * 2;
            *(u32*)(up + 0 * CV_WAVE) = f16_pk(bflo(a[it].x), bflo(bq[it].x));
            *(u32*)(up + 1 * CV_WAVE) = f16_pk(bfhi(a[it].x), bfhi(bq[it].x));
            *(u32*)(up + 2 * CV_WAVE) = f16_pk(bflo(a[it].y), bflo(bq[it].y));
            *(u32*)(up + 3 * CV_WAVE) = f16_pk(bfhi(a[it].y), bfhi(bq[it].y));
        }
    }
    __syncthreads();
    const int bc = lane & 15, kq = lane >> 4;
    const char* fa = my + (bc & 3) * CV_COPY + 2 * (2032 - 4 * (bc >> 2) + 8 * kq) - 64 * 63;
    const char* ga = my + 4 * CV_COPY + 2 * (16 + 16 * bc + 8 * kq);
    f32x4 acc[8];
#pragma unroll
    for (int t = 0; t < 8; ++t) acc[t] = f32x4{};
#pragma unroll
    for (int mu = 0; mu < 8; ++mu) {
        f16x8 G[8], F[8];
#pragma unroll
        for (int t = 0; t < 8; ++t) G[t] = *(const f16x8*)(ga + 512 * t + 64 * (7 - mu));
#pragma unroll
        for (int lam = 0; lam < 8; ++lam) {
            const char* q = fa + 64 * (63 - (mu + 8 * lam));
            const u32x2 lo = *(const u32x2*)q, hi = *(const u32x2*)(q + 8);
            F[lam] = __builtin_bit_cast(f16x8, u32x4{lo.x, lo.y, hi.x, hi.y});
        }
#pragma unroll
        for (int lam = 0; lam < 8; ++lam)
#pragma unroll
            for (int t = 0; t + lam < 8; ++t) acc[t + lam] = __builtin_amdgcn_mfma_f32_16x16x32_f16(F[lam], G[t], acc[t + lam], 0, 0, 0);
    }
    const float sc = ((const float*)(ws + WS_KSC))[ch];
    u16* yt = (u16*)(ws + WS_YT) + (size_t)ch * L_;
#pragma unroll
    for (int t = 0; t < 8; ++t)
        *(u32x2*)(yt + 256 * t + 16 * bc + 4 * kq) = u32x2{cvt_pk_bf16(acc[t][0] * sc, acc[t][1] * sc), cvt_pk_bf16(acc[t][2] * sc, acc[t][3] * sc)};
}

constexpr int G2_LDS = 60 * 1024;

template <int NK> DEV void g2_main(f32x4 (&acc)[5][4], const char* hb, const char* wb, u32 loff) {
    u32x4 hf[3][4], wf[3][5];
    auto issue = [&](auto kc) {
        constexpr int k = decltype(kc)::value, sl = k % 3;
        static_for<0, 4>([&](auto ic) { constexpr int i = decltype(ic)::value; gload16<0>(hf[sl][i], loff, hb + (size_t)i * 54 * 1024 + k * 1024); });
        static_for<0, 5>([&](auto ic) { constexpr int i = decltype(ic)::value; gload16<0>(wf[sl][i], loff, wb + (size_t)i * 54 * 1024 + k * 1024); });
    };
    issue(IC(0)); issue(IC(1));
    static_for<0, NK>([&](auto kc) {
        constexpr int k = decltype(kc)::value, sl = k % 3;
        if constexpr (k + 2 < NK) issue(IC(k + 2));
        constexpr int after = (k + 2 < NK ? 18 : (k + 1 < NK ? 9 : 0));
        asm volatile("s_waitcnt vmcnt(%9)" : "+v"(hf[sl][0]), "+v"(hf[sl][1]), "+v"(hf[sl][2]), "+v"(hf[sl][3]), "+v"(wf[sl][0]), "+v"(wf[sl][1]), "+v"(wf[sl][2]),
                     "+v"(wf[sl][3]), "+v"(wf[sl][4]) : "i"(after));
#pragma unroll
        for (int nt = 0; nt < 5; ++nt)
#pragma unroll
            for (int rt = 0; rt < 4; ++rt)
                acc[nt][rt] = __builtin_amdgcn_mfma_f32_16x16x32_bf16(__builtin_bit_cast(bf16x8, wf[sl][nt]), __builtin_bit_cast(bf16x8, hf[sl][rt]), acc[nt][rt], 0, 0, 0);
        __builtin_amdgcn_sched_barrier(0);
    });
}

__global__ __launch_bounds__(256) void k_gemm2(const u16* __restrict__ x, const u16* __restrict__ dskip, char* ws, u16* __restrict__ out) {
    extern __shared__ __attribute__((aligned(16))) char smem[];
    const int tid = threadIdx.x, lane = tid & 63, wave = __builtin_amdgcn_readfirstlane(tid >> 6);
    const int b = blockIdx.x, xcd = b & 7, idx = b >> 3;
    const int mt = (xcd >> 1) * 8 + (idx & 7), nb = (xcd & 1) * 4 + (idx >> 3);
    const int r0 = mt * 64, n0 = nb * 80;
    const int k0 = wave < 2 ? wave * 14 : 28 + (wave - 2) * 13;
    const char* hb = ws + WS_H + ((size_t)(mt * 4) * 54 + k0) * 1024;
    const char* wb = ws + WS_W2P + ((size_t)(nb * 5) * 54 + k0) * 1024;
    const int r = r0 + 16 * wave + (lane & 15), q = lane >> 4;
    u32x2 xv[5], xnv[5], dv[5]; u32 yv[5][4];
    const u16* xnp = (const u16*)(ws + WS_XNP);
    const u16* yt = (const u16*)(ws + WS_YT);
#pragma unroll
    for (int nt = 0; nt < 5; ++nt) {
        const int n = n0 + 16 * nt + 4 * q;
        xv[nt] = *(const u32x2*)(x + (size_t)r * D_ + n);
        xnv[nt] = *(const u32x2*)(xnp + ((size_t)((r >> 5) * 40 + (n >> 4)) * 64 + (r & 31) + 32 * ((n >> 3) & 1)) * 8 + (n & 7));
        dv[nt] = *(const u32x2*)(dskip + n);
#pragma unroll
        for (int j = 0; j < 4; ++j) yv[nt][j] = yt[(size_t)(n + j) * L_ + r];
    }
    asm volatile("s_waitcnt vmcnt(0)" ::: "memory");
    f32x4 acc[5][4];
#pragma unroll
    for (int nt = 0; nt < 5; ++nt)
#pragma unroll
        for (int rt = 0; rt < 4; ++rt) acc[nt][rt] = f32x4{};
    if (wave < 2) g2_main<14>(acc, hb, wb, lane * 16); else g2_main<13>(acc, hb, wb, lane * 16);
#pragma unroll
    for (int rt = 0; rt < 4; ++rt) {
        if (rt != wave) {
            const int sl = (wave - rt - 1) & 3;
#pragma unroll
            for (int nt = 0; nt < 5; ++nt) *(f32x4*)(smem + ((rt * 3 + sl) * 5 + nt) * 1024 + lane * 16) = acc[nt][rt];
        }
    }
    __syncthreads();
    f32x4 res[5];
#pragma unroll
    for (int nt = 0; nt < 5; ++nt) {
        f32x4 own = wave == 0 ? acc[nt][0] : wave == 1 ? acc[nt][1] : wave == 2 ? acc[nt][2] : acc[nt][3];
#pragma unroll
        for (int sl = 0; sl < 3; ++sl) own += *(const f32x4*)(smem + ((wave * 3 + sl) * 5 + nt) * 1024 + lane * 16);
        res[nt] = own;
    }
#pragma unroll
    for (int nt = 0; nt < 5; ++nt) {
        const int n = n0 + 16 * nt + 4 * q;
        float o[4];
#pragma unroll
        for (int j = 0; j < 4; ++j) {
            const float xv_ = (j & 1) ? bfhi(xv[nt][j >> 1]) : bflo(xv[nt][j >> 1]);
            const float xn_ = (j & 1) ? bfhi(xnv[nt][j >> 1]) : bflo(xnv[nt][j >> 1]);
            const float d_ = (j & 1) ? bfhi(dv[nt][j >> 1]) : bflo(dv[nt][j >> 1]);
            const float a = rbf(bf2f(yv[nt][j]) + rbf(xn_ * d_));
            o[j] = rbf(xv_ + a) + rbf(res[nt][j]);
        }
        *(u32x2*)(out + (size_t)r * D_ + n) = u32x2{cvt_pk_bf16(o[0], o[1]), cvt_pk_bf16(o[2], o[3])};
    }
}

extern "C" void kernel_launch(void* const* d_in, const int* in_sizes, int n_in, void* d_out, int out_size, void* d_ws, size_t ws_size,
                              hipStream_t stream) {
    const u16* u = (const u16*)d_in[0];
    char* ws = (char*)d_ws;
    hipFuncSetAttribute((const void*)k_gemm1, hipFuncAttributeMaxDynamicSharedMemorySize, G1_LDS);
    hipFuncSetAttribute((const void*)k_conv, hipFuncAttributeMaxDynamicSharedMemorySize, CV_LDS);
    hipFuncSetAttribute((const void*)k_prep, hipFuncAttributeMaxDynamicSharedMemorySize, PREP_LDS);
    hipFuncSetAttribute((const void*)k_gemm2, hipFuncAttributeMaxDynamicSharedMemorySize, G2_LDS);
    k_prep<<<PREP_KG_BLOCKS + PREP_LN_BLOCKS + PREP_W2_BLOCKS, 256, PREP_LDS, stream>>>(u, (const u16*)d_in[1], (const u16*)d_in[2], (const u16*)d_in[3],
                                                                                         (const u16*)d_in[4], (const u16*)d_in[5], (const u16*)d_in[10], ws);
    k_gemm1<<<256, 256, G1_LDS, stream>>>((const u16*)d_in[8], (const u16*)d_in[9], ws);
    k_conv<<<160, 256, CV_LDS, stream>>>(ws);
    k_gemm2<<<256, 256, G2_LDS, stream>>>(u, (const u16*)d_in[6], ws, (u16*)d_out);
}
```

```cpp
#include <hip/hip_runtime.h>
#include <hip/hip_bf16.h>
#include <stdint.h>
#include <type_traits>

typedef unsigned short u16;
typedef unsigned u32;
typedef u32 u32x2 __attribute__((ext_vector_type(2)));
typedef u32 u32x4 __attribute__((ext_vector_type(4)));
typedef float f32x2 __attribute__((ext_vector_type(2)));
typedef float f32x4 __attribute__((ext_vector_type(4)));
typedef float f32x16 __attribute__((ext_vector_type(16)));
typedef short s16x4 __attribute__((ext_vector_type(4)));
typedef short s16x8 __attribute__((ext_vector_type(8)));
typedef __bf16 bf16x8 __attribute__((ext_vector_type(8)));
typedef _Float16 f16x8 __attribute__((ext_vector_type(8)));
typedef _Float16 f16x2 __attribute__((ext_vector_type(2)));
typedef __amdgpu_buffer_rsrc_t srd_t;
#define LAS __attribute__((address_space(3)))
#define DEV __device__ __forceinline__
#define IC(n) std::integral_constant<int, (n)>{}

constexpr int L_ = 2048, D_ = 640, F_ = 1728, N_ = 128;

constexpr size_t WS_XNP = 0;
constexpr int KR_ROW = 2096;
constexpr size_t WS_KR  = WS_XNP + (size_t)L_ * D_ * 2;
constexpr size_t WS_KSC = WS_KR + (size_t)D_ * KR_ROW * 2;
constexpr size_t WS_W2P = WS_KSC + 4096;
constexpr size_t WS_H   = WS_W2P + (size_t)F_ * D_ * 2;
constexpr size_t WS_YT  = WS_H + (size_t)L_ * F_ * 2;
constexpr size_t WS_DATA_END = WS_YT + (size_t)L_ * D_ * 2;
constexpr size_t WS_FLAG = (WS_DATA_END + 4095) & ~(size_t)4095;
constexpr int FL_A = 0, FL_A_REP = 1024, FL_W2 = 8192, FL_W2_REP = 512, FL_H = 12288, FL_Y = 14336, FL_BYTES = 16384;
constexpr size_t WS_EPOCH = WS_FLAG + FL_BYTES;
constexpr size_t WS_END = WS_EPOCH + 256;

DEV float bf2f(u32 b) { return __uint_as_float(b << 16); }
DEV float bflo(u32 w) { return __uint_as_float(w << 16); }
DEV float bfhi(u32 w) { return __uint_as_float(w & 0xffff0000u); }
typedef __bf16 bf16x2 __attribute__((ext_vector_type(2)));
DEV u32 cvt_pk_bf16(float lo, float hi) { return __builtin_bit_cast(u32, __builtin_convertvector(f32x2{lo, hi}, bf16x2)); }
DEV float rbf(float x) { return bflo(cvt_pk_bf16(x, x)); }
DEV u32 f16_pk(float lo, float hi) { f16x2 v = {(_Float16)lo, (_Float16)hi}; return __builtin_bit_cast(u32, v); }
template <int I, int N, class Fn> DEV void static_for(Fn&& f) { if constexpr (I < N) { f(IC(I)); static_for<I + 1, N>(f); } }
DEV u32 rfl(u32 v) { return __builtin_amdgcn_readfirstlane(v); }
DEV srd_t make_srd(const void* p, u32 bytes) {
    const size_t a = (size_t)p;
    const size_t u = (size_t)rfl((u32)a) | ((size_t)rfl((u32)(a >> 32)) << 32);
    return __builtin_amdgcn_make_buffer_rsrc((void*)u, (short)0, (int)rfl(bytes), 0x00020000);
}
DEV u32x4 srd4(const void* p, u32 bytes) {
    const size_t a = (size_t)p;
    return u32x4{rfl((u32)a), rfl((u32)(a >> 32)) & 0xffffu, rfl(bytes), 0x00020000u};
}
#define SCHED() __builtin_amdgcn_sched_barrier(0)
template <int N> DEV void wait_lgkm() { __builtin_amdgcn_s_waitcnt(0xC07F | (N << 8)); }
template <int N> DEV void wait_vm() { asm volatile("s_waitcnt vmcnt(%0)" :: "i"(N) : "memory"); }
DEV void barrier() { asm volatile("" ::: "memory"); __builtin_amdgcn_s_barrier(); asm volatile("" ::: "memory"); }
template <int OFF, class S> DEV void bload16(u32x4& dst, u32 voff, S srd, u32 soff) {
    asm volatile("buffer_load_dwordx4 %0, %1, %2, %3 offen offset:%4" : "=v"(dst) : "v"(voff), "s"(srd), "s"(soff), "i"(OFF));
}
template <int OFF, class S> DEV void bload8(u32x2& dst, u32 voff, S srd, u32 soff) {
    asm volatile("buffer_load_dwordx2 %0, %1, %2, %3 offen offset:%4" : "=v"(dst) : "v"(voff), "s"(srd), "s"(soff), "i"(OFF));
}
template <int OFF, class S> DEV void bload16_sc1(u32x4& dst, u32 voff, S srd, u32 soff) {
    asm volatile("buffer_load_dwordx4 %0, %1, %2, %3 offen offset:%4 sc1" : "=v"(dst) : "v"(voff), "s"(srd), "s"(soff), "i"(OFF));
}
template <int OFF, class S> DEV void bload8_sc1(u32x2& dst, u32 voff, S srd, u32 soff) {
    asm volatile("buffer_load_dwordx2 %0, %1, %2, %3 offen offset:%4 sc1" : "=v"(dst) : "v"(voff), "s"(srd), "s"(soff), "i"(OFF));
}
DEV void bstore16_sc1(u32x4 v, srd_t srd, u32 voff) { __builtin_amdgcn_raw_buffer_store_b128(v, srd, voff, 0, 16); }
DEV void drain_vm() { asm volatile("s_waitcnt vmcnt(0)" ::: "memory"); }
DEV void flag_store(char* ws, int off, int idx, u32 tag) {
    __hip_atomic_store((__attribute__((address_space(1))) u32*)(ws + WS_FLAG + off + idx * 4), tag, __ATOMIC_RELAXED, __HIP_MEMORY_SCOPE_AGENT);
}
DEV void flag_store_rep(char* ws, int off, int rep, int idx, u32 tag) {
    const int lane = threadIdx.x & 63;
    if (lane < 8) flag_store(ws, off + lane * rep, idx, tag);
}
template <int N> DEV bool wait_flags(char* ws, int off, int first, u32 tag) {
    const int lane = threadIdx.x & 63;
    const srd_t sf = make_srd(ws + WS_FLAG + off, 1024);
#pragma unroll 1
    for (int it = 0; it < (1 << 18); ++it) {
        bool ok;
        if constexpr (N <= 64) {
            u32 v;
            asm volatile("buffer_load_dword %0, %1, %2, 0 offen sc1\n\ts_waitcnt vmcnt(0)" : "=&v"(v) : "v"((u32)(first + (lane < N ? lane : 0)) * 4), "s"(sf) : "memory");
            ok = v == tag;
        } else {
            u32x4 v;
            const int l = 4 * lane < N ? lane : 0;
            asm volatile("buffer_load_dwordx4 %0, %1, %2, 0 offen sc1\n\ts_waitcnt vmcnt(0)" : "=&v"(v) : "v"((u32)(first + 4 * l) * 4), "s"(sf) : "memory");
            ok = v.x == tag && v.y == tag && v.z == tag && v.w == tag;
        }
        if (__all(ok)) return true;
        __builtin_amdgcn_s_sleep(4);
    }
    return false;
}
template <int N> DEV void flags_request(u32& v, char* ws, int off, int first, bool on) {
    const int lane = threadIdx.x & 63;
    asm volatile("buffer_load_dword %0, %1, %2, 0 offen sc1" : "=v"(v) : "v"((u32)(first + (lane < N ? lane : 0)) * 4), "s"(make_srd(ws + WS_FLAG + off, on ? 1024 : 0)));
}
template <int N> DEV void flags_request(u32x4& v, char* ws, int off, int first, bool on) {
    const int lane = threadIdx.x & 63;
    asm volatile("buffer_load_dwordx4 %0, %1, %2, 0 offen sc1" : "=v"(v) : "v"((u32)(first + 4 * (4 * lane < N ? lane : 0)) * 4), "s"(make_srd(ws + WS_FLAG + off, on ? 1024 : 0)));
}
DEV bool flags_ok(u32 v, u32 tag) { return __all(v == tag); }
DEV bool flags_ok(const u32x4& v, u32 tag) { return __all(v.x == tag && v.y == tag && v.z == tag && v.w == tag); }
DEV void set_m0r(u32 base, u32 off) { asm volatile("s_add_i32 m0, %0, %1" :: "s"(base), "s"(off) : "m0", "scc"); }
DEV void group_sync(u32 lds_word) {
    asm volatile("s_waitcnt lgkmcnt(0)" ::: "memory");
    if ((threadIdx.x & 63) == 0) __hip_atomic_fetch_add((LAS u32*)(size_t)lds_word, 1u, __ATOMIC_RELAXED, __HIP_MEMORY_SCOPE_WORKGROUP);
    while (rfl(*(volatile LAS u32*)(size_t)lds_word) < 4) __builtin_amdgcn_s_sleep(1);
    asm volatile("" ::: "memory");
}
DEV void verdict_set(u32 lds_word, bool ok) { if ((threadIdx.x & 63) == 0) *(volatile LAS u32*)(size_t)lds_word = ok ? 1u : 2u; }
DEV bool verdict_get(u32 lds_word) {
    u32 v;
    while ((v = rfl(*(volatile LAS u32*)(size_t)lds_word)) == 0) __builtin_amdgcn_s_sleep(1);
    asm volatile("" ::: "memory");
    return v == 1;
}
template <int IMM> DEV void set_m0(u32 base) { asm volatile("s_add_i32 m0, %0, %1" :: "s"(base), "i"(IMM) : "m0", "scc"); }
template <class S> DEV void bdma16_sc1(u32 voff, S srd, u32 soff) { asm volatile("buffer_load_dwordx4 %0, %1, %2 offen sc1 lds" :: "v"(voff), "s"(srd), "s"(soff) : "memory"); }
template <class S> DEV void bdma16(u32 voff, S srd, u32 soff) { asm volatile("buffer_load_dwordx4 %0, %1, %2 offen lds" :: "v"(voff), "s"(srd), "s"(soff) : "memory"); }
DEV void mfma16_f16(f32x4& c, f16x8 a, f16x8 b) { asm volatile("v_mfma_f32_16x16x32_f16 %0, %1, %2, %0" : "+v"(c) : "v"(a), "v"(b)); }
DEV void mfma16_bf16(f32x4& c, u32x4 a, u32x4 b) { asm volatile("v_mfma_f32_16x16x32_bf16 %0, %1, %2, %0" : "+v"(c) : "v"(a), "v"(b)); }
DEV void mfma_settle() { asm volatile("s_nop 15" ::: "memory"); }
DEV s16x4 tr_read(u32 addr) { return __builtin_bit_cast(s16x4, __builtin_amdgcn_ds_read_tr16_b64_v4i16((LAS s16x4*)(size_t)addr)); }

#define STAMP_DECL(kid)
#define ST()
#define STAMP_FLUSH()
#define STPARAM
#define STARGS
#define KST()
constexpr int KG_ROW = 528;
constexpr int KG_R0 = 21 * KG_ROW;
constexpr int KG_TBL = 53 * KG_ROW;
constexpr int KG_WAVE = KG_TBL + 1360;

struct GatherTbl {
    u16 v[2560];
    constexpr GatherTbl() : v() {
        for (int t = 0; t < 2560; ++t) {
            int idx = 641;
            if (t >= 1 && t <= 2048) {
                const int l = 2048 - t;
                int e = 0; while ((l >> e) >= 256) ++e;
                int m = l >> e; const int rem = l - (m << e), half = e ? 1 << (e - 1) : 0;
                if (e && (rem > half || (rem == half && (m & 1)))) ++m;
                idx = m + 128 * e;
            }
            v[t] = (u16)(2 * idx);
        }
    }
};
__device__ const GatherTbl g_gather = GatherTbl();

#define DPP_F(v, ctrl) __builtin_bit_cast(float, __builtin_amdgcn_update_dpp(0, __builtin_bit_cast(int, v), ctrl, 0xf, 0xf, true))
DEV float row16_sum(float v) {
    v += DPP_F(v, 0xB1); v += DPP_F(v, 0x4E); v += DPP_F(v, 0x141); v += DPP_F(v, 0x140);
    return v;
}
DEV float wave_max(float v) {
    v = fmaxf(v, DPP_F(v, 0xB1)); v = fmaxf(v, DPP_F(v, 0x4E)); v = fmaxf(v, DPP_F(v, 0x141)); v = fmaxf(v, DPP_F(v, 0x140));
    const auto s = __builtin_amdgcn_permlane16_swap(__float_as_uint(v), __float_as_uint(v), false, false);
    v = fmaxf(__uint_as_float(s[0]), __uint_as_float(s[1]));
    const auto t = __builtin_amdgcn_permlane32_swap(__float_as_uint(v), __float_as_uint(v), false, false);
    return fmaxf(__uint_as_float(t[0]), __uint_as_float(t[1]));
}
DEV void lds_fence() { asm volatile("s_waitcnt lgkmcnt(0)" ::: "memory"); }
DEV f32x2 unpk(u32 w) { return f32x2{bflo(w), bfhi(w)}; }
DEV float exp_acc(float x) {
    const float t = x * 1.4426950408889634f;
    const float r = __builtin_fmaf(x, 1.4426950408889634f, -t) + x * 1.925963033500e-8f;
    const float e = __builtin_amdgcn_exp2f(t);
    return __builtin_fmaf(e * r, 0.6931471805599453f, e);
}

DEV void ln_rows(const u32x4 (&v)[5], int r, int l16, char* ws) {
    f32x2 s2 = {0.f, 0.f};
#pragma unroll
    for (int i = 0; i < 5; ++i)
#pragma unroll
        for (int j = 0; j < 4; ++j) s2 += unpk(v[i][j]);
    const float mean = row16_sum(s2.x + s2.y) * (1.0f / D_);
    f32x2 q2 = {0.f, 0.f};
#pragma unroll
    for (int i = 0; i < 5; ++i)
#pragma unroll
        for (int j = 0; j < 4; ++j) { const f32x2 a = unpk(v[i][j]) - mean; q2 += a * a; }
    const float q = row16_sum(q2.x + q2.y);
    const float m = rbf(mean);
    const float var = rbf(q * (1.0f / D_));
    const float sd = rbf(__builtin_amdgcn_sqrtf(rbf(var + 1e-5f)));
    const float rs = __builtin_amdgcn_rcpf(sd);
    const srd_t so = make_srd(ws + WS_XNP, L_ * D_ * 2);
#pragma unroll
    for (int i = 0; i < 5; ++i) {
        u32x4 o;
#pragma unroll
        for (int j = 0; j < 4; ++j) {
            const f32x2 a = unpk(v[i][j]) - m;
            const f32x2 c = unpk(cvt_pk_bf16(a.x, a.y)) * rs;
            o[j] = cvt_pk_bf16(c.x, c.y);
        }
        const int chi = l16 + 16 * i;
        bstore16_sc1(o, so, (u32)(((r >> 5) * 40 + (chi >> 1)) * 64 + (r & 31) + 32 * (chi & 1)) * 16);
    }
}

DEV void cmul2(f32x2& r0, f32x2 a0, f32x2 b0, f32x2& r1, f32x2 a1, f32x2 b1) {
    asm("v_pk_mul_f32 %0, %2, %3 op_sel:[0,0] op_sel_hi:[0,1]\n\tv_pk_mul_f32 %1, %4, %5 op_sel:[0,0] op_sel_hi:[0,1]\n\t"
        "v_pk_fma_f32 %0, %2, %3, %0 op_sel:[1,1,0] op_sel_hi:[1,0,1] neg_lo:[0,1,0]\n\tv_pk_fma_f32 %1, %4, %5, %1 op_sel:[1,1,0] op_sel_hi:[1,0,1] neg_lo:[0,1,0]"
        : "=&v"(r0), "=&v"(r1) : "v"(a0), "v"(b0), "v"(a1), "v"(b1));
}
struct c2 { f32x2 v[2]; };
DEV c2 cmul(const c2& a, const c2& b) { c2 r; cmul2(r.v[0], a.v[0], b.v[0], r.v[1], a.v[1], b.v[1]); return r; }
DEV f32x2 cexp_p(float e2, float tr, float p) {
    const float mag = __builtin_amdgcn_exp2f(e2 * p);
    const float th = __builtin_amdgcn_fractf(tr * p);
    return f32x2{mag * __builtin_amdgcn_cosf(th), mag * __builtin_amdgcn_sinf(th)};
}

struct KgIn { u32 ldt, la[2], ai[2], c_r[2], c_i[2]; u32x4 tb[5]; };
DEV void kgen_wave(const KgIn& in, char* scr, u16* krow, float& ksc) {
    const int lane = threadIdx.x & 63;
    const float dt = rbf(exp_acc(bf2f(in.ldt)));
    c2 C;
#pragma unroll
    for (int hf = 0; hf < 2; ++hf) C.v[hf] = f32x2{bf2f(in.c_r[hf]), bf2f(in.c_i[hf])} * dt;
    const float mx = wave_max(fmaxf(fmaxf(fabsf(C.v[0].x), fabsf(C.v[0].y)), fmaxf(fabsf(C.v[1].x), fabsf(C.v[1].y))));
    int ex = (int)((__float_as_uint(mx) >> 23) & 0xff);
    ex = ex < 1 ? 127 : (ex > 253 ? 253 : ex);
    const float sig = __uint_as_float((u32)(254 - ex) << 23);
    ksc = 2.0f * __uint_as_float((u32)ex << 23);
    c2 w1, w8, w128, q;
#pragma unroll
    for (int hf = 0; hf < 2; ++hf) {
        const float zr = -rbf(exp_acc(bf2f(in.la[hf]))) * dt, zi = bf2f(in.ai[hf]) * dt;
        const float e2 = zr * 1.4426950408889634f, tr = zi * 0.15915494309189535f;
        w1.v[hf] = cexp_p(e2, tr, 1.f); w8.v[hf] = cexp_p(e2, tr, 8.f); w128.v[hf] = cexp_p(e2, tr, 128.f); q.v[hf] = cexp_p(e2, tr, 256.f);
    }
    {
        const c2 w16 = cmul(w8, w8);
        c2 r0{{f32x2{1.f, 0.f}, f32x2{1.f, 0.f}}}, r1 = w8;
        char* p = scr + KG_R0 + lane * 8;
#pragma unroll 2
        for (int j = 0; j < 8; ++j) {
            const c2 h0 = cmul(r0, w128), h1 = cmul(r1, w128), n0 = cmul(r0, w16), n1 = cmul(r1, w16);
            auto put = [&](int row, const c2& v) { *(u32x2*)(p + row * KG_ROW) = u32x2{f16_pk(v.v[0].x, v.v[0].y), f16_pk(v.v[1].x, v.v[1].y)}; };
            put(0, r0); put(1, r1); put(16, h0); put(17, h1);
            r0 = n0; r1 = n1; p += 2 * KG_ROW;
        }
    }
    auto putL = [&](auto ic, const c2& v) { constexpr int i = decltype(ic)::value;
        *(u32x2*)(scr + i * KG_ROW + lane * 8) = u32x2{f16_pk(v.v[0].x, -v.v[0].y), f16_pk(v.v[1].x, -v.v[1].y)}; };
    {
        c2 l[8];
        l[0].v[0] = C.v[0] * sig; l[0].v[1] = C.v[1] * sig;
        const c2 q2 = cmul(q, q);
        putL(IC(0), l[0]);
        static_for<1, 8>([&](auto ic) { constexpr int i = decltype(ic)::value; l[i] = cmul(l[i - 1], w1); putL(ic, l[i]); });
        const c2 q3 = cmul(q2, q), a = cmul(q2, l[0]);
        static_for<0, 4>([&](auto ic) { constexpr int i = decltype(ic)::value; putL(IC(8 + i), cmul(q, l[2 * i])); });
        putL(IC(12), a); putL(IC(13), cmul(q2, l[4])); putL(IC(14), cmul(q3, l[0])); putL(IC(15), cmul(q3, l[4]));
        c2 c = cmul(q2, a);
        static_for<0, 5>([&](auto ic) { constexpr int i = decltype(ic)::value; putL(IC(16 + i), c); if (i < 4) c = cmul(c, q); });
    }
    f32x16 acc = {};
    const int fi = lane & 31, fh = lane >> 5;
    const char* fa = scr + fi * KG_ROW + 16 * fh;
    lds_fence();
#pragma unroll
    for (int hf = 0; hf < 2; ++hf) {
        f16x8 a[8], bb[8];
#pragma unroll
        for (int ks = 0; ks < 8; ++ks) { a[ks] = *(const f16x8*)(fa + 256 * hf + 32 * ks); bb[ks] = *(const f16x8*)(fa + KG_R0 + 256 * hf + 32 * ks); }
#pragma unroll
        for (int ks = 0; ks < 8; ++ks) acc = __builtin_amdgcn_mfma_f32_32x32x16_f16(a[ks], bb[ks], acc, 0, 0, 0);
    }
    u16* T = (u16*)(scr + KG_TBL);
    static_for<0, 12>([&](auto rc) {
        constexpr int reg = decltype(rc)::value;
        const int r3 = reg & 3, g = reg >> 2, j = fi;
        int idx;
        if (g == 0) idx = r3 + 4 * fh + 8 * j;
        else if (g == 1) idx = fh == 0 ? 256 + r3 + 4 * j : (384 + 64 * (r3 >> 1) + (r3 & 1) + 2 * j);
        else idx = fh == 0 ? 512 + 32 * r3 + j : (r3 == 0 && j == 0 ? 640 : 642);
        const _Float16 v = (_Float16)acc[reg];
        T[idx] = __builtin_bit_cast(u16, v);
    });
    if (lane == 0) T[641] = 0;
    lds_fence();
    const char* Tb = (const char*)T;
    u32 lo[20], hi[20], gv[20];
#pragma unroll
    for (int i = 0; i < 20; ++i) { const u32 o = in.tb[i >> 2][i & 3]; lo[i] = *(const u16*)(Tb + (o & 0xffffu)); hi[i] = *(const u16*)(Tb + (o >> 16)); }
    SCHED();
#pragma unroll
    for (int i = 0; i < 20; ++i) gv[i] = lo[i] | (hi[i] << 16);
#pragma unroll
    for (int i = 0; i < 5; ++i) {
        const u32x4 g = {gv[4 * i], gv[4 * i + 1], gv[4 * i + 2], gv[4 * i + 3]};
        __builtin_amdgcn_raw_buffer_store_b128(g, make_srd(krow, KR_ROW * 2), (i < 4 || lane < 4) ? (u32)(8 + 8 * (lane + 64 * i)) * 2 : 0x40000000u, 0, 16);
    }
}

DEV u32 w2_chunk_off(int unit, int i) {
    const int c = (threadIdx.x & 63) + 64 * i, row = (c * 3277) >> 16, col = c - 20 * row;
    return (u32)(((unit >> 2) * 32 + row) * D_ + (unit & 3) * 160 + col * 8) * 2;
}
DEV void w2pack_wave(const u32x4 (&v)[10], int unit, char* scr, char* ws) {
    const int lane = threadIdx.x & 63;
    const int ks = unit >> 2, nq = unit & 3;
    constexpr int RS = 320 + 32;
#pragma unroll
    for (int i = 0; i < 10; ++i) { const int c = lane + 64 * i, row = (c * 3277) >> 16, col = c - 20 * row; *(u32x4*)(scr + row * RS + col * 16) = v[i]; }
    lds_fence();
    const int kq = lane >> 4, q = (lane & 15) >> 2, p = lane & 3;
    const u32 a0 = (u32)(size_t)(LAS char*)scr + (16 * (kq >> 1) + 4 * (kq & 1) + q) * RS + 8 * p;
    const srd_t so = make_srd(ws + WS_W2P, F_ * D_ * 2);
#pragma unroll
    for (int t = 0; t < 10; ++t) {
        const s16x4 lo = tr_read(a0 + 32 * t), hi = tr_read(a0 + 8 * RS + 32 * t);
        const s16x8 w = __builtin_shufflevector(lo, hi, 0, 1, 2, 3, 4, 5, 6, 7);
        bstore16_sc1(__builtin_bit_cast(u32x4, w), so, (u32)(((nq * 10 + t) * 54 + ks) * 64 + lane) * 16);
    }
}
constexpr int G1_UOFF = 14336 + 128, G1_STAGE = G1_UOFF + 14336, G1_NST = 5, G1_LDS = G1_STAGE * G1_NST;
#define WARM_SLEEP 1
constexpr int G1_TILE = 64 * F_ * 2;

struct G1Load {
    u32x4 s1, s3; u32 voff[7], mb[7]; bool up3;
    DEV void setup(char* smem, const u16* w1, const u16* w3, int ft, int w) {
        const int lane = threadIdx.x & 63, f0 = ft * 112;
        const int nvc = (F_ - f0) >= 112 ? 14 : (F_ - f0) / 8;
        const u32 lds0 = (u32)(size_t)(LAS char*)smem;
        s1 = srd4(w1 + f0, (u32)(D_ * F_ - f0) * 2); s3 = srd4(w3 + f0, (u32)(D_ * F_ - f0) * 2);
#pragma unroll
        for (int i = 0; i < 7; ++i) {
            const int g = w + 4 * i, up = g >= 14, p = up ? g - 14 : g;
            const int c = 64 * p + lane, row = (c * 4682) >> 16; int col = c - 14 * row; col = col < nvc ? col : nvc - 1;
            voff[i] = row * (F_ * 2) + col * 16;
            mb[i] = rfl(lds0 + (up ? G1_UOFF : 0) + p * 1024);
        }
        up3 = w >= 2;
    }
    DEV void tile(u32 dst, u32 src, bool paced) const {
        static_for<0, 7>([&](auto ic) { constexpr int i = decltype(ic)::value;
            set_m0r(mb[i], dst); asm volatile("s_nop 0");
            if constexpr (i < 3) bdma16(voff[i], s1, src); else if constexpr (i > 3) bdma16(voff[i], s3, src); else bdma16(voff[i], up3 ? s3 : s1, src);
            if (paced) __builtin_amdgcn_s_sleep(1);
        });
    }
    template <int i> DEV void dma(u32 src) const {
        if constexpr (i < 3) bdma16(voff[i], s1, src); else if constexpr (i > 3) bdma16(voff[i], s3, src); else bdma16(voff[i], up3 ? s3 : s1, src);
    }
};

DEV void g1_warm(u32 dump, const u16* w1, const u16* w3, int ft, int j, int part) {
    const int lane = threadIdx.x & 63, f0 = ft * 112;
    const int nvc = (F_ - f0) >= 112 ? 14 : (F_ - f0) / 8;
    const u32x4 s = srd4((part ? w3 : w1) + f0, (u32)(D_ * F_ - f0) * 2);
    const int r = (lane * 4682) >> 16; int col = lane - 14 * r; col = col < nvc ? col : nvc - 1;
    const u32 vo = lane < 56 ? (u32)((j + 8 * r) * (F_ * 2) + col * 16) : 0x40000000u;
    asm volatile("s_mov_b32 m0, %0\n\ts_nop 0" :: "s"(dump) : "m0");
#pragma unroll 1
    for (u32 so = 0; so < 20 * 32 * F_ * 2; so += 32 * F_ * 2) { bdma16(vo, s, so); __builtin_amdgcn_s_sleep(WARM_SLEEP); }
}
DEV void g1_tiles(char* smem, const u16* w1, const u16* w3, int ft, int part, int t0, int t1) {
    const int lane = threadIdx.x & 63, f0 = ft * 112;
    const int nvc = (F_ - f0) >= 112 ? 14 : (F_ - f0) / 8;
    const u32x4 s = srd4((part ? w3 : w1) + f0, (u32)(D_ * F_ - f0) * 2);
    const u32 base = rfl((u32)(size_t)(LAS char*)smem + (part ? G1_UOFF : 0));
    u32 vo[14];
#pragma unroll
    for (int p = 0; p < 14; ++p) { const int c = 64 * p + lane, row = (c * 4682) >> 16; int cc = c - 14 * row; cc = cc < nvc ? cc : nvc - 1; vo[p] = (u32)(row * (F_ * 2) + cc * 16); }
#pragma unroll 1
    for (int t = t0; t < t1; ++t)
        static_for<0, 14>([&](auto pc) { constexpr int p = decltype(pc)::value; set_m0r(base, t * G1_STAGE + p * 1024); asm volatile("s_nop 0"); bdma16(vo[p], s, t * G1_TILE); });
}

struct G1X { u32x4 xf[8]; u32x4 sx; };
DEV void g1_x_open(G1X& X, char* ws, int mt, int wave) {
    X.sx = srd4(ws + WS_XNP + (size_t)(mt * 4 + wave) * 40 * 1024, 40 * 1024);
    static_for<0, 8>([&](auto sc) { constexpr int s = decltype(sc)::value; bload16_sc1<(s & 3) * 1024>(X.xf[s], (threadIdx.x & 63) * 16, X.sx, (s >> 2) * 4096); });
}
DEV void g1_mfma_loop(const G1Load& g, G1X& X, char* smem, f32x16 (&acc)[7] STPARAM) {
    const int lane = threadIdx.x & 63;
    u32x4 (&xf)[8] = X.xf;
    const u32 xoff = lane * 16;
    const u32 tb = (u32)(size_t)(LAS char*)smem + ((lane >> 4) & 1) * G1_UOFF + (8 * (lane >> 5) + ((lane & 15) >> 2)) * 224 + 8 * (lane & 3);
#pragma unroll
    for (int T = 0; T < 7; ++T) acc[T] = f32x16{};
    s16x4 wr[2][14];
    u32 cur = tb, nxt = tb + G1_STAGE;
    u32 rst = 1;
    u32 dst = 4 * G1_STAGE, dsrc = 4 * G1_TILE;
    u32 xso = 2 * 4096;
    static_for<0, 14>([&](auto ic) { constexpr int i = decltype(ic)::value; wr[0][i] = tr_read(cur + 4 * (i & 1) * 224 + 32 * (i >> 1)); });
    asm volatile("s_waitcnt vmcnt(4)" : "+v"(xf[0]), "+v"(xf[1]), "+v"(xf[2]), "+v"(xf[3]) :: "memory");
    ST();
    auto trip = [&](auto dmac, auto xlc) {
        constexpr bool DMA = decltype(dmac)::value, XL = decltype(xlc)::value;
        static_for<0, 2>([&](auto hc) {
            constexpr int h = decltype(hc)::value;
            static_for<0, 4>([&](auto kc) {
                constexpr int ks = decltype(kc)::value, par = ks & 1;
                const bf16x8 xb8 = __builtin_bit_cast(bf16x8, xf[4 * h + ks]);
                auto rd = [&](auto ic) { constexpr int i = decltype(ic)::value;
                    wr[par ^ 1][i] = tr_read((ks < 3 ? cur + 16 * (ks + 1) * 224 : nxt) + 4 * (i & 1) * 224 + 32 * (i >> 1)); };
                static_for<0, 7>([&](auto Tc) {
                    constexpr int T = decltype(Tc)::value;
                    if constexpr (T == 0) wait_lgkm<6>();
                    if constexpr (T == 4) wait_lgkm<12>();
                    SCHED();
                    const s16x8 w = __builtin_shufflevector(wr[par][2 * T], wr[par][2 * T + 1], 0, 1, 2, 3, 4, 5, 6, 7);
                    acc[T] = __builtin_amdgcn_mfma_f32_32x32x16_bf16(__builtin_bit_cast(bf16x8, w), xb8, acc[T], 0, 0, 0);
                    SCHED();
                    if constexpr (T < 4) { rd(IC(3 * T)); rd(IC(3 * T + 1)); rd(IC(3 * T + 2)); }
                    if constexpr (T == 4) { rd(IC(12)); rd(IC(13)); if constexpr (DMA) set_m0r(g.mb[2 * ks], dst); }
                    if constexpr (DMA && T == 5) { g.template dma<2 * ks>(dsrc); if constexpr (2 * ks + 1 < 7) set_m0r(g.mb[2 * ks + 1], dst); }
                    if constexpr (DMA && T == 6 && 2 * ks + 1 < 7) g.template dma<2 * ks + 1>(dsrc);
                    if constexpr (XL && T == 6) bload16_sc1<ks * 1024>(xf[4 * h + ks], xoff, X.sx, xso);
                    SCHED();
                });
            });
            asm volatile("s_waitcnt vmcnt(%4)" : "+v"(xf[4 * (h ^ 1)]), "+v"(xf[4 * (h ^ 1) + 1]), "+v"(xf[4 * (h ^ 1) + 2]), "+v"(xf[4 * (h ^ 1) + 3])
                         : "i"((DMA ? 7 : 0) + (XL ? 4 : 0)) : "memory");
            wait_lgkm<14>();
            barrier();
            cur = nxt; rst = rst == G1_NST - 1 ? 0 : rst + 1; nxt = tb + rst * G1_STAGE; xso += 4096;
            if constexpr (DMA) { dst = dst == (G1_NST - 1) * G1_STAGE ? 0 : dst + G1_STAGE; dsrc += G1_TILE; }
        });
    };
#pragma unroll 1
    for (int it = 0; it < 3; ++it) trip(std::true_type{}, std::true_type{});
    trip(std::false_type{}, std::true_type{});
    trip(std::false_type{}, std::false_type{});
    wait_lgkm<0>();
    barrier();
}
DEV void g1_epilogue(const f32x16 (&acc)[7], char* ws, int mt, int ft) {
    const int lane = threadIdx.x & 63, wave = rfl(threadIdx.x >> 6);
    const int r32 = lane & 31, hh = lane >> 5;
    const int rt16 = (mt * 128 + wave * 32 + r32) >> 4;
    const srd_t sh = make_srd(ws + WS_H, L_ * F_ * 2);
#pragma unroll
    for (int T = 0; T < 7; ++T) {
        const int Tg = ft * 7 + T;
        u32x4 o;
#pragma unroll
        for (int j = 0; j < 4; ++j) {
            const f32x2 gt = {acc[T][2 * j], acc[T][2 * j + 1]}, u = {acc[T][8 + 2 * j], acc[T][8 + 2 * j + 1]};
            const f32x2 e = gt * -1.4426950408889634f;
            const f32x2 d = f32x2{__builtin_amdgcn_exp2f(e.x), __builtin_amdgcn_exp2f(e.y)} + 1.0f;
            const f32x2 hv = (gt * u) * f32x2{__builtin_amdgcn_rcpf(d.x), __builtin_amdgcn_rcpf(d.y)};
            o[j] = cvt_pk_bf16(hv.x, hv.y);
        }
        bstore16_sc1(o, sh, Tg < 108 ? (u32)((rt16 * 54 + (Tg >> 1)) * 64 + (r32 & 15) + 16 * (2 * (Tg & 1) + hh)) * 16 : 0x40000000u);
    }
}
constexpr int CV_COPY = 4256, CV_UOFF = 17 * 1024, CV_U = 4608, CV_WAVE = CV_UOFF + CV_U, CV_BASE = 64, CV_LDS = CV_BASE + 4 * CV_WAVE;
struct Aux {
    u32x4 v[10];
    DEV void load(char* ws, int ch) {
        const u32x4 s = srd4(ws + WS_KR + (size_t)ch * KR_ROW * 2, KR_ROW * 2);
        static_for<0, 10>([&](auto ic) { constexpr int i = decltype(ic)::value; bload16<(i < 5 ? 16 : 0)>(v[i], (u32)(threadIdx.x & 63) * 16, s, (i % 5) * 1024); });
    }
    template <int N> DEV void wait() {
        asm volatile("s_waitcnt vmcnt(%10)" : "+v"(v[0]), "+v"(v[1]), "+v"(v[2]), "+v"(v[3]), "+v"(v[4]), "+v"(v[5]), "+v"(v[6]), "+v"(v[7]), "+v"(v[8]), "+v"(v[9]) : "i"(N));
    }
    template <int k> DEV void put(char* smem) const {
        constexpr int c = k & 3, i = k >> 2;
        const int lane = threadIdx.x & 63, wave = rfl(threadIdx.x >> 6) & 3;
        const u32x4 a = v[i], q = v[5 + i];
        const u32x4 o = c == 0 ? a : c == 2 ? u32x4{q.w, a.x, a.y, a.z}
                      : c == 1 ? u32x4{__builtin_amdgcn_alignbit(a.x, q.w, 16), __builtin_amdgcn_alignbit(a.y, a.x, 16), __builtin_amdgcn_alignbit(a.z, a.y, 16), __builtin_amdgcn_alignbit(a.w, a.z, 16)}
                               : u32x4{__builtin_amdgcn_alignbit(q.w, q.z, 16), __builtin_amdgcn_alignbit(a.x, q.w, 16), __builtin_amdgcn_alignbit(a.y, a.x, 16), __builtin_amdgcn_alignbit(a.z, a.y, 16)};
        const int ch16 = lane + 64 * i;
        *(u32x4*)(smem + CV_BASE + wave * CV_WAVE + (ch16 < 266 ? c * CV_COPY + 16 * ch16 : 4 * CV_COPY + 16 * (lane & 15))) = o;
    }
};
struct ConvU { u32x2 a[4], b[4]; };
template <int i> DEV void conv_load_u1(ConvU& u, char* ws, int cb) {
    const int tid = threadIdx.x & 255, c0 = cb * 4;
    const int ks = c0 >> 4, hf = (c0 >> 3) & 1, j0 = c0 & 7;
    const u32x4 sx = srd4(ws + WS_XNP, L_ * D_ * 2);
    const u32 o = (u32)(((((2 * tid) >> 5) * 40 + ks) * 64 + ((2 * tid) & 31) + 32 * hf) * 8 + j0) * 2;
    if constexpr (i & 1) bload8_sc1<16>(u.b[i >> 1], o, sx, (i >> 1) * 16 * 40 * 1024); else bload8_sc1<0>(u.a[i >> 1], o, sx, (i >> 1) * 16 * 40 * 1024);
}
DEV void conv_pin_u(ConvU& u) { asm volatile("" : "+v"(u.a[0]), "+v"(u.a[1]), "+v"(u.a[2]), "+v"(u.a[3]), "+v"(u.b[0]), "+v"(u.b[1]), "+v"(u.b[2]), "+v"(u.b[3])); }

DEV void conv_stage_u(char* smem0, const ConvU& u) {
    const int tid = threadIdx.x & 255;
    char* smem = smem0 + CV_BASE;
    if (tid < 128) *(u32x4*)(smem + (tid >> 5) * CV_WAVE + CV_UOFF + (tid & 31) * 16) = u32x4{0, 0, 0, 0};
#pragma unroll
    for (int it = 0; it < 4; ++it) {
        char* up = smem + CV_UOFF + (256 + it * 512 + 2 * tid) * 2;
        *(u32*)(up + 0 * CV_WAVE) = f16_pk(bflo(u.a[it].x), bflo(u.b[it].x));
        *(u32*)(up + 1 * CV_WAVE) = f16_pk(bfhi(u.a[it].x), bfhi(u.b[it].x));
        *(u32*)(up + 2 * CV_WAVE) = f16_pk(bflo(u.a[it].y), bflo(u.b[it].y));
        *(u32*)(up + 3 * CV_WAVE) = f16_pk(bfhi(u.a[it].y), bfhi(u.b[it].y));
    }
}
struct ConvLoop {
    u32 fa[2], ga; f16x8 G[2][8], F[2][8];
    template <int c, int d, int i> DEV void rd() {
        typedef u32x4 u32x4a8 __attribute__((aligned(8)));
        if constexpr (i >= 1 && i < 9) G[c][i - 1] = *(const LAS f16x8*)(size_t)(ga + 512 * (i - 1) + 64 * d);
        else { constexpr int lam = i < 1 ? 0 : i - 8;
            F[c][lam] = __builtin_bit_cast(f16x8, *(const LAS u32x4a8*)(size_t)(fa[(7 - lam) >> 2] + 512 * ((7 - lam) & 3) + 64 * d)); }
    }
    DEV void begin(char* smem0, f32x4 (&acc)[8]) {
        const int lane = threadIdx.x & 63, wave = rfl(threadIdx.x >> 6) & 3, bc = lane & 15, kq = lane >> 4;
        const u32 my32 = (u32)(size_t)(LAS char*)smem0 + CV_BASE + wave * CV_WAVE;
        ga = my32 + CV_UOFF + 2 * (16 + 16 * bc + 8 * kq);
        fa[0] = my32 + (bc & 3) * CV_COPY + 2 * (2032 - 4 * (bc >> 2) + 8 * kq) - 64 * 63; fa[1] = fa[0] + 2048;
#pragma unroll
        for (int t = 0; t < 8; ++t) acc[t] = f32x4{};
        asm volatile("" : "+v"(acc[0]), "+v"(acc[1]), "+v"(acc[2]), "+v"(acc[3]), "+v"(acc[4]), "+v"(acc[5]), "+v"(acc[6]), "+v"(acc[7]));
        static_for<0, 16>([&](auto ic) { rd<0, 0, decltype(ic)::value>(); });
    }
    template <class Hook> DEV void trip(f32x4 (&acc)[8], Hook&& hook) {
        static_for<0, 2>([&](auto cc) {
            constexpr int c = decltype(cc)::value;
            wait_lgkm<0>();
            SCHED();
            static_for<0, 36>([&](auto ic) {
                constexpr int i = decltype(ic)::value;
                constexpr int lam = i < 8 ? 0 : i < 15 ? 1 : i < 21 ? 2 : i < 26 ? 3 : i < 30 ? 4 : i < 33 ? 5 : i < 35 ? 6 : 7;
                constexpr int t = i - (lam == 0 ? 0 : lam == 1 ? 8 : lam == 2 ? 15 : lam == 3 ? 21 : lam == 4 ? 26 : lam == 5 ? 30 : lam == 6 ? 33 : 35);
                mfma16_f16(acc[t + lam], F[c][lam], G[c][t]);
                SCHED();
                if constexpr ((i & 1) == 0 && i / 2 < 16) rd<c ^ 1, c + 1, i / 2>();
                hook(IC(36 * c + i));
                SCHED();
            });
        });
        fa[0] += 128; fa[1] += 128; ga += 128;
        asm volatile("" : "+v"(fa[0]), "+v"(fa[1]), "+v"(ga));
    }
    DEV void end(f32x4 (&acc)[8]) {
        wait_lgkm<0>();
        SCHED(); mfma_settle(); asm volatile("" : "+v"(acc[0]), "+v"(acc[1]), "+v"(acc[2]), "+v"(acc[3]), "+v"(acc[4]), "+v"(acc[5]), "+v"(acc[6]), "+v"(acc[7]));
    }
};
DEV void conv_finish(char* smem0, char* ws, int cb, float sc, float dsk, const f32x4 (&acc)[8], u32 sync_word) {
    const int tid = threadIdx.x & 255, lane = tid & 63, wave = rfl(tid >> 6), bc = lane & 15, kq = lane >> 4;
    char* smem = smem0 + CV_BASE;
    char* my = smem + wave * CV_WAVE;
#pragma unroll
    for (int t = 0; t < 8; ++t) {
        const int l = 256 * t + 16 * bc + 4 * kq;
        typedef _Float16 f16x4 __attribute__((ext_vector_type(4)));
        const f16x4 uv = *(const f16x4*)(my + CV_UOFF + 2 * (256 + l));
        *(u32x2*)(my + 2 * l) = u32x2{cvt_pk_bf16(acc[t][0] * sc + dsk * (float)uv[0], acc[t][1] * sc + dsk * (float)uv[1]),
                                      cvt_pk_bf16(acc[t][2] * sc + dsk * (float)uv[2], acc[t][3] * sc + dsk * (float)uv[3])};
    }
    group_sync(sync_word);
    const srd_t sy = make_srd(ws + WS_YT + (size_t)cb * (L_ * 8), L_ * 8);
#pragma unroll
    for (int j = 0; j < 4; ++j) {
        const int p = j * 256 + tid;
        const u32 y0 = *(const u32*)(smem + 0 * CV_WAVE + p * 4), y1 = *(const u32*)(smem + 1 * CV_WAVE + p * 4);
        const u32 y2 = *(const u32*)(smem + 2 * CV_WAVE + p * 4), y3 = *(const u32*)(smem + 3 * CV_WAVE + p * 4);
        bstore16_sc1(u32x4{__builtin_amdgcn_perm(y1, y0, 0x05040100), __builtin_amdgcn_perm(y3, y2, 0x05040100),
                           __builtin_amdgcn_perm(y1, y0, 0x07060302), __builtin_amdgcn_perm(y3, y2, 0x07060302)}, sy, (u32)p * 16);
    }
}
#define G2_WARM 1
#define KST2()
constexpr int G2_LDS = 60 * 1024;
template <int N> DEV void wait9(u32x4 (&f)[9]) {
    asm volatile("s_waitcnt vmcnt(%9)" : "+v"(f[0]), "+v"(f[1]), "+v"(f[2]), "+v"(f[3]), "+v"(f[4]), "+v"(f[5]), "+v"(f[6]), "+v"(f[7]), "+v"(f[8]) : "i"(N));
}
DEV void pin4(f32x4 (&a)[4]) { asm volatile("" : "+v"(a[0]), "+v"(a[1]), "+v"(a[2]), "+v"(a[3])); }
DEV void keep9(const u32x4 (&f)[9]) { asm volatile("" :: "v"(f[0]), "v"(f[1]), "v"(f[2]), "v"(f[3]), "v"(f[4]), "v"(f[5]), "v"(f[6]), "v"(f[7]), "v"(f[8])); }

struct G2 {
    u32x4 sw; u32 vo[9], so; u32x4 fr[3][9];
    DEV void setup(char* ws, int mt, int nb) {
        const int lane = threadIdx.x & 63, wave = rfl(threadIdx.x >> 6);
        sw = srd4(ws, (u32)WS_DATA_END);
#pragma unroll
        for (int i = 0; i < 5; ++i) vo[i] = (u32)WS_W2P + (nb * 5 + i) * 54 * 1024 + lane * 16;
#pragma unroll
        for (int i = 0; i < 4; ++i) vo[5 + i] = (u32)WS_H + (mt * 4 + i) * 54 * 1024 + lane * 16;
        so = (wave < 2 ? wave * 14 : 28 + (wave - 2) * 13) * 1024;
    }
    u32x2 ex[5];
    DEV void open_w2(const u16* x, int mt, int nb, int mtl, u32 dump) {
        const int lane = threadIdx.x & 63, wave = rfl(threadIdx.x >> 6);
        static_for<0, 10>([&](auto ic) { constexpr int i = decltype(ic)::value; bload16_sc1<(i / 5) * 1024>(fr[i / 5][i % 5], vo[i % 5], sw, so); });
        const u32x4 sx = srd4(x + nb * 80, (u32)(L_ * D_ - nb * 80) * 2);
        const u32 xo = (u32)((mt * 64 + 16 * wave + (lane & 15)) * D_ + 4 * (lane >> 4)) * 2;
        static_for<0, 5>([&](auto ic) { constexpr int nt = decltype(ic)::value; bload8<nt * 32>(ex[nt], xo, sx, 0); });
        asm volatile("s_mov_b32 m0, %0\n\ts_nop 0" :: "s"(dump) : "m0");
#pragma unroll 1
        for (int p = wave; p < 35; p += 4) {
            const int j = (p * 13) >> 6, t = p - 5 * j, k = mtl + 8 * j;
            bdma16_sc1((u32)WS_W2P + (nb * 5 + t) * 54 * 1024 + lane * 16, sw, k < 54 ? (u32)k * 1024 : 0x70000000u);
        }
    }
    DEV void open_h() { static_for<0, 8>([&](auto ic) { constexpr int i = decltype(ic)::value; bload16_sc1<(i / 4) * 1024>(fr[i / 4][5 + i % 4], vo[5 + i % 4], sw, so); }); }

    DEV void run(char* smem, char* ws, u16* __restrict__ out, int mt, int nb, u32 tag, u32 yword, u32 sync_word, bool& alive STPARAM) {
        const int tid = threadIdx.x, lane = tid & 63, wave = rfl(tid >> 6);
        const int r0 = mt * 64, n0 = nb * 80;
        const int r = r0 + 16 * wave + (lane & 15), q = lane >> 4;
        u32x2 ey[5];
        const u32 yo = (u32)WS_YT + (u32)((n0 / 4 + q) * L_ + r) * 8;
        KST2();
        f32x4 acc[5][4];
#pragma unroll
        for (int nt = 0; nt < 5; ++nt)
#pragma unroll
            for (int rt = 0; rt < 4; ++rt) acc[nt][rt] = f32x4{};
        static_for<0, 5>([&](auto ic) { pin4(acc[decltype(ic)::value]); });
        auto step = [&](auto slc, auto&& hook) {
            constexpr int sl = decltype(slc)::value;
            static_for<0, 20>([&](auto ic) {
                constexpr int i = decltype(ic)::value, nt = i / 4, rt = i % 4;
                mfma16_bf16(acc[nt][rt], fr[sl][nt], fr[sl][5 + rt]);
                SCHED();
                hook(ic);
                SCHED();
            });
        };
        wait9<4>(fr[0]);
        so += 2048; u32 last = wave < 2 ? 0xffffffffu : so + 11 * 1024;
#pragma unroll 1
        for (int it = 0; it < 4; ++it) {
            static_for<0, 3>([&](auto sc) {
                constexpr int sl = decltype(sc)::value, ns = (sl + 2) % 3;
                wait9<9>(fr[sl]);
                const u32 se = so == last ? 0x70000000u : so;
                step(sc, [&](auto ic) { constexpr int i = decltype(ic)::value;
                    if constexpr ((i & 1) == 0 && i / 2 < 9) bload16_sc1<0>(fr[ns][(i / 2) % 9], vo[(i / 2) % 9], sw, se); });
                so += 1024;
            });
        }
        u32 yfl;
        wait9<9>(fr[0]);
        step(IC(0), [&](auto ic) { if constexpr (decltype(ic)::value == 0) flags_request<20>(yfl, ws, FL_Y + 128 * nb, 0, wave == 0); });
        wait9<1>(fr[1]);
        asm volatile("s_waitcnt vmcnt(0)" : "+v"(yfl) :: "memory");
        if (wave == 0) verdict_set(yword, flags_ok(yfl, tag) || wait_flags<20>(ws, FL_Y + 128 * nb, 0, tag));
        alive &= verdict_get(yword);
        step(IC(1), [&](auto ic) { constexpr int i = decltype(ic)::value;
            if constexpr (i < 10 && (i & 1) == 0) bload8_sc1<0>(ey[(i / 2) % 5], yo, sw, (i / 2) * 4 * L_ * 8); });
        SCHED(); mfma_settle(); static_for<0, 5>([&](auto ic) { pin4(acc[decltype(ic)::value]); });
        ST();
#pragma unroll
        for (int rt = 0; rt < 4; ++rt) {
            if (rt != wave) {
                const int sl = (wave - rt - 1) & 3;
#pragma unroll
                for (int nt = 0; nt < 5; ++nt) *(f32x4*)(smem + ((rt * 3 + sl) * 5 + nt) * 1024 + lane * 16) = acc[nt][rt];
            }
        }
        group_sync(sync_word);
        f32x4 res[5];
#pragma unroll
        for (int nt = 0; nt < 5; ++nt) {
            f32x4 own = wave == 0 ? acc[nt][0] : wave == 1 ? acc[nt][1] : wave == 2 ? acc[nt][2] : acc[nt][3];
#pragma unroll
            for (int sl = 0; sl < 3; ++sl) own += *(const f32x4*)(smem + ((wave * 3 + sl) * 5 + nt) * 1024 + lane * 16);
            res[nt] = own;
        }
        KST2();
        asm volatile("s_waitcnt vmcnt(0)" : "+v"(ey[0]), "+v"(ey[1]), "+v"(ey[2]), "+v"(ey[3]), "+v"(ey[4]), "+v"(ex[0]), "+v"(ex[1]), "+v"(ex[2]), "+v"(ex[3]), "+v"(ex[4]));
#pragma unroll
        for (int nt = 0; nt < 5; ++nt) {
            const int n = n0 + 16 * nt + 4 * q;
            float o[4];
#pragma unroll
            for (int j = 0; j < 4; ++j) {
                const float xv_ = (j & 1) ? bfhi(ex[nt][j >> 1]) : bflo(ex[nt][j >> 1]);
                const float y_ = (j & 1) ? bfhi(ey[nt][j >> 1]) : bflo(ey[nt][j >> 1]);
                o[j] = (xv_ + y_) + res[nt][j];
                if (!alive) o[j] = __builtin_nanf("");
            }
            *(u32x2*)(out + (size_t)r * D_ + n) = u32x2{cvt_pk_bf16(o[0], o[1]), cvt_pk_bf16(o[2], o[3])};
        }
    }
};
constexpr unsigned CODE_BYTES = 30 * 1024;
#define LB_W 2
constexpr int LDS_SCR = G1_STAGE;
constexpr int LDS_G2 = CV_LDS + 192;
constexpr int LDS_CTL = 156 * 1024;
constexpr int LDS_DUMP = LDS_CTL + 64;
constexpr int FUSED_LDS = LDS_DUMP + 1024;
static_assert(LDS_SCR + 4 * KG_WAVE <= LDS_CTL && G1_LDS <= LDS_CTL && LDS_G2 + G2_LDS <= LDS_CTL && LDS_G2 % 16 == 0 && FUSED_LDS <= 160 * 1024, "lds");
enum { C_LN = 0, C_ROWS = 4, C_ALLROWS = 8, C_H = 12, C_Y = 16, C_SYNC_H = 20, C_SYNC_RED = 24, C_SYNC_U = 28, C_SYNC_YT = 32, C_SYNC_PUB = 36, C_TAG = 40, C_SYNC_KG = 44, C_OUT = 48, C_XIN = 52, C_W2 = 56 };

template <int OFF> DEV void bload_u16(u32& dst, u32 voff, u32x4 srd) {
    asm volatile("buffer_load_ushort %0, %1, %2, 0 offen offset:%3" : "=v"(dst) : "v"(voff), "s"(srd), "i"(OFF));
}
DEV u32 load_tag(char* ws) {
    u32 epoch;
    asm volatile("buffer_load_dword %0, off, %1, 0 sc1\n\ts_waitcnt vmcnt(0)" : "=&v"(epoch) : "s"(srd4(ws + WS_EPOCH, 4)) : "memory");
    return rfl(epoch) * 0x9E3779B1u + 0x7F4A7C15u;
}

extern "C" __global__ __launch_bounds__(512, LB_W) void k_fused(const u16* __restrict__ x, const u16* __restrict__ logA, const u16* __restrict__ Aim, const u16* __restrict__ Cre,
                                                  const u16* __restrict__ Cim, const u16* __restrict__ logdt, const u16* __restrict__ dskip,
                                                  const u16* __restrict__ w1, const u16* __restrict__ w3, const u16* __restrict__ w2, char* ws, u16* __restrict__ out) {
    extern __shared__ __attribute__((aligned(16))) char smem[];
    const int tid = threadIdx.x, lane = tid & 63, wave = rfl(tid >> 6);
    const int b = blockIdx.x, xcd = b & 7, idx = b >> 3;
    const bool cvb = b < 160;
    STAMP_DECL(0) ST();
    const int mt = (xcd & 1) * 8 + (idx & 7), ft = (xcd >> 1) * 4 + (idx >> 3);
    const u32 ctl = (u32)(size_t)(LAS char*)smem + LDS_CTL;
    bool alive = true;
    if (tid < 16) *(volatile LAS u32*)(size_t)(ctl + 4 * tid) = 0;
    lds_fence();
    barrier();
    if (wave < 4) {
        G1X X;
        if (wave < 2) {
            u32x4 xr[5];
            const int lrow = b * 8 + wave * 4 + (lane >> 4);
            const u32x4 sx = srd4(x, L_ * D_ * 2);
            static_for<0, 5>([&](auto ic) { constexpr int i = decltype(ic)::value; bload16<256 * i>(xr[i], (u32)(lrow * D_ + 8 * (lane & 15)) * 2, sx, 0); });
            const u32 tag = load_tag(ws);
            asm volatile("" : "+v"(xr[0]), "+v"(xr[1]), "+v"(xr[2]), "+v"(xr[3]), "+v"(xr[4]));
            if (tid == 0) *(volatile LAS u32*)(size_t)(ctl + C_TAG) = tag;
            if (wave == 0) verdict_set(ctl + C_XIN, true);
            __builtin_amdgcn_s_setprio(2);
            ln_rows(xr, lrow, lane & 15, ws);
            drain_vm();
            u32 prev = 0;
            if (lane == 0) prev = __hip_atomic_fetch_add((LAS u32*)(size_t)(ctl + C_LN), 1u, __ATOMIC_RELAXED, __HIP_MEMORY_SCOPE_WORKGROUP);
            if (rfl(prev) == 1) { flag_store_rep(ws, FL_A, FL_A_REP, b, tag); verdict_set(ctl + C_OUT, true); }
            __builtin_amdgcn_s_setprio(0);
            if (wave == 0) verdict_set(ctl + C_ROWS, wait_flags<16>(ws, FL_A + xcd * FL_A_REP, 16 * mt, tag));
        } else {
            if (wave == 2) {
                u32 lo, hi;
                asm volatile("s_getpc_b64 s[30:31]\n.Lpf_%=:\n\ts_sub_u32 %0, s30, .Lpf_%=-k_fused\n\ts_subb_u32 %1, s31, 0" : "=s"(lo), "=s"(hi) :: "s30", "s31", "scc");
                const u32x4 sc = {lo, hi & 0xffffu, CODE_BYTES, 0x00020000u};
                asm volatile("s_mov_b32 m0, %0\n\ts_nop 0" :: "s"((u32)(size_t)(LAS char*)smem + LDS_DUMP) : "m0");
                bdma16((u32)(idx * 1024 + lane * 16), sc, 0);
            }
            verdict_get(ctl + C_XIN);
            g1_warm((u32)(size_t)(LAS char*)smem + LDS_DUMP, w1, w3, ft, idx & 7, wave & 1);
            verdict_get(ctl + C_OUT);
            g1_tiles(smem, w1, w3, ft, wave & 1, 0, 1);
        }
        ST();
        alive &= verdict_get(ctl + C_ROWS);
        g1_x_open(X, ws, mt, wave);
        G1Load g; g.setup(smem, w1, w3, ft, wave);
        wait_vm<8>();
        ST();
        lds_fence();
        barrier();
        const u32 tag = rfl(*(volatile LAS u32*)(size_t)(ctl + C_TAG));
        f32x16 acc[7];
        g1_mfma_loop(g, X, smem, acc STARGS);
        ST();
        g1_epilogue(acc, ws, mt, ft);
        ST();
        drain_vm();
        group_sync(ctl + C_SYNC_H);
        if (tid == 0) flag_store(ws, FL_H + 128 * mt, ft, tag);
        ST();
        const int mt2 = (xcd >> 1) * 8 + (idx & 7), nb = (xcd & 1) * 4 + (idx >> 3);
        if (wave == 0) verdict_set(ctl + C_W2, wait_flags<96>(ws, FL_W2 + xcd * FL_W2_REP, 0, tag));
        G2 g2; g2.setup(ws, mt2, nb);
        alive &= verdict_get(ctl + C_W2);
        g2.open_w2(x, mt2, nb, idx & 7, (u32)(size_t)(LAS char*)smem + LDS_DUMP);
        if (wave == 0) verdict_set(ctl + C_H, wait_flags<16>(ws, FL_H + 128 * (mt2 >> 1), 0, tag));
        alive &= verdict_get(ctl + C_H);
        ST();
        g2.open_h();
        g2.run(smem + LDS_G2, ws, out, mt2, nb, tag, ctl + C_Y, ctl + C_SYNC_RED, alive STARGS);
        ST();
    } else {
        const int w = wave - 4, ch = b * 4 + w;
        float ksc = 0.f;
        KgIn in;
        {
            const u32 o = (u32)(ch * N_ + lane) * 2, n = cvb ? D_ * N_ * 2 : 0;
            const u32x4 sa = srd4(logA, n), sb = srd4(Aim, n), sc = srd4(Cre, n), sd = srd4(Cim, n), st = srd4(&g_gather, cvb ? sizeof(GatherTbl) : 0);
            bload_u16<0>(in.ldt, (u32)ch * 2, srd4(logdt, cvb ? D_ * 2 : 0));
            bload_u16<0>(in.la[0], o, sa); bload_u16<128>(in.la[1], o, sa); bload_u16<0>(in.ai[0], o, sb); bload_u16<128>(in.ai[1], o, sb);
            bload_u16<0>(in.c_r[0], o, sc); bload_u16<128>(in.c_r[1], o, sc); bload_u16<0>(in.c_i[0], o, sd); bload_u16<128>(in.c_i[1], o, sd);
            static_for<0, 5>([&](auto ic) { constexpr int i = decltype(ic)::value; bload16<(i & 3) * 1024>(in.tb[i], (u32)lane * 16, st, (i >> 2) * 4096); });
        }
        drain_vm();
        asm volatile("" : "+v"(in.ldt), "+v"(in.la[0]), "+v"(in.la[1]), "+v"(in.ai[0]), "+v"(in.ai[1]), "+v"(in.c_r[0]), "+v"(in.c_r[1]),
                     "+v"(in.c_i[0]), "+v"(in.c_i[1]), "+v"(in.tb[0]), "+v"(in.tb[1]), "+v"(in.tb[2]), "+v"(in.tb[3]), "+v"(in.tb[4]));
        if (cvb) kgen_wave(in, smem + LDS_SCR + w * KG_WAVE, (u16*)(ws + WS_KR) + (size_t)ch * KR_ROW, ksc);
        else {
            const int unit = (3 - w) * 96 + (b - 160);
            verdict_get(ctl + C_OUT);
            if (unit < 216) {
                u32x4 v[10];
#pragma unroll
                for (int i = 0; i < 10; ++i) v[i] = *(const u32x4*)((const char*)w2 + w2_chunk_off(unit, i));
                w2pack_wave(v, unit, smem + LDS_SCR + w * KG_WAVE, ws);
            }
        }
        G1Load ld; ld.setup(smem, w1, w3, ft, w);
        ST();
        group_sync(ctl + C_SYNC_KG);
        verdict_get(ctl + C_OUT);
        ld.tile(1 * G1_STAGE, 1 * G1_TILE, false);
        wait_vm<0>();
        if (!cvb) {
            group_sync(ctl + C_SYNC_PUB);
            if (w == 0) flag_store_rep(ws, FL_W2, FL_W2_REP, b - 160, rfl(*(volatile LAS u32*)(size_t)(ctl + C_TAG)));
        }
        ST();
        barrier();
        const u32 tag = rfl(*(volatile LAS u32*)(size_t)(ctl + C_TAG));
        ld.tile(2 * G1_STAGE, 2 * G1_TILE, false); ld.tile(3 * G1_STAGE, 3 * G1_TILE, false);
        wait_vm<7>();
        barrier();
        wait_vm<0>();
        barrier();
#pragma unroll 1
        for (int t = 2; t < 6; ++t) barrier();
        if (cvb && w == 0) {
            const bool ok = wait_flags<256>(ws, FL_A + xcd * FL_A_REP, 0, tag);
            verdict_set(ctl + C_ALLROWS, ok);
            if (ok && b == 0 && lane == 0)
                __hip_atomic_store((__attribute__((address_space(1))) u32*)(ws + WS_EPOCH), tag, __ATOMIC_RELAXED, __HIP_MEMORY_SCOPE_AGENT);
        }
        barrier();
        barrier(); barrier(); barrier();
        barrier();
        if (cvb) {
            alive &= verdict_get(ctl + C_ALLROWS);
            Aux aux; ConvU cu;
            aux.load(ws, ch);
            static_for<0, 8>([&](auto jc) { conv_load_u1<decltype(jc)::value>(cu, ws, b); });
            u32 dsk_bits;
            bload_u16<0>(dsk_bits, (u32)ch * 2, srd4(dskip, D_ * 2));
            aux.wait<9>();
            ST();
            static_for<0, 20>([&](auto ic) { aux.put<decltype(ic)::value>(smem); });
            drain_vm();
            conv_pin_u(cu);
            asm volatile("" : "+v"(dsk_bits));
            conv_stage_u(smem, cu);
            group_sync(ctl + C_SYNC_U);
            ST();
            f32x4 yacc[8]; ConvLoop cl;
            cl.begin(smem, yacc);
#pragma unroll 1
            for (int it = 0; it < 4; ++it) cl.trip(yacc, [](auto) {});
            cl.end(yacc);
            ST();
            if (!alive) ksc = __builtin_nanf("");
            conv_finish(smem, ws, b, ksc, bf2f(dsk_bits), yacc, ctl + C_SYNC_YT);
            drain_vm();
            group_sync(ctl + C_SYNC_PUB);
            if (tid == 256) flag_store(ws, FL_Y + 128 * (b / 20), b % 20, tag);
            ST();
        }
    }
    STAMP_FLUSH();
}

extern "C" void kernel_launch(void* const* d_in, const int* in_sizes, int n_in, void* d_out, int out_size, void* d_ws, size_t ws_size,
                              hipStream_t stream) {
    (void)hipFuncSetAttribute((const void*)k_fused, hipFuncAttributeMaxDynamicSharedMemorySize, FUSED_LDS);
    k_fused<<<256, 512, FUSED_LDS, stream>>>((const u16*)d_in[0], (const u16*)d_in[1], (const u16*)d_in[2], (const u16*)d_in[3], (const u16*)d_in[4],
                                             (const u16*)d_in[5], (const u16*)d_in[6], (const u16*)d_in[8], (const u16*)d_in[9], (const u16*)d_in[10],
                                             (char*)d_ws, (u16*)d_out);
}
```
